```python
import torch

def _softmax(x, axis=-1):
    return torch.softmax(x, dim=axis)
def _gelu(x):
    return torch.nn.functional.gelu(x, approximate="tanh")
def _silu(x):
    return torch.nn.functional.silu(x)
def _sigmoid(x):
    return torch.sigmoid(x)
def _layernorm(x, axis=-1, eps=1e-5):
    m = x.mean(dim=axis, keepdim=True)
    v = x.var(dim=axis, keepdim=True, unbiased=False)
    return (x - m) / torch.sqrt(v + eps)
def _scatter_add(tgt, idx, src, axis=0):
    return tgt.clone().index_add_(axis, idx, src)
def _segment_sum(src, seg, n):
    out = torch.zeros((n,) + src.shape[1:], dtype=src.dtype)
    return out.index_add_(0, seg, src)
def _pad(x, pw):
    flat = tuple(v for pair in reversed(pw) for v in pair)
    return torch.nn.functional.pad(x, flat)

kernel_name = 'gated_mix__ssm_conv_s4d__ssm_conv_h3__prompt'

def setup_inputs():

    _w = (lambda a: (lambda t: t.to(torch.bfloat16) if t.is_floating_point() else t)(torch.as_tensor(a)))
    def _A_setup():
        Bsz, L, D, N = 1, 2048, 512, 128
        return {
            "u": _w((lambda *s: torch.randn(*s, dtype=torch.bfloat16) * 0.05)(Bsz, L, D)),
            "log_A_real": _w((lambda *s: torch.randn(*s, dtype=torch.bfloat16) * 3.2)(D, N) * 0.5),
            "A_imag": _w((lambda *s: torch.randn(*s, dtype=torch.bfloat16) * 0.2)(D, N) * 3.14),
            "C_real": _w((lambda *s: torch.randn(*s, dtype=torch.bfloat16) * 3.2)(D, N) / N ** 0.5),
            "C_imag": _w((lambda *s: torch.randn(*s, dtype=torch.bfloat16) * 3.2)(D, N) / N ** 0.5),
            "log_dt": _w((lambda *s: torch.randn(*s, dtype=torch.bfloat16) * 0.2)(D) * 0.5 - 4.0),
            "D_skip": _w((lambda *s: torch.randn(*s, dtype=torch.bfloat16) * 0.4)(D)),
        }
    _A_in = _A_setup()

    def _B_setup():
        B, S, D = 1, 2048, 512
        ks = 4
        return {
            "q": _w((lambda *s: torch.randn(*s, dtype=torch.bfloat16) * 0.05)(B, S, D)),
            "k": _w((lambda *s: torch.randn(*s, dtype=torch.bfloat16) * 0.8)(B, S, D)),
            "v": _w((lambda *s: torch.randn(*s, dtype=torch.bfloat16) * 0.8)(B, S, D)),
            "shift_k": _w((lambda *s: torch.randn(*s, dtype=torch.bfloat16) * 0.8)(D, ks) * 0.5),
            "ssm_k": _w((lambda *s: torch.randn(*s, dtype=torch.bfloat16) * 0.8)(D, S) * (1.0 / S ** 0.5)),
            "ssm_D": _w((lambda *s: torch.randn(*s, dtype=torch.bfloat16) * 0.8)(D)),
        }
    _B_in = _B_setup()

    return {
        "A_u": _A_in["u"],
        "A_log_A_real": _A_in["log_A_real"],
        "A_A_imag": _A_in["A_imag"],
        "A_C_real": _A_in["C_real"],
        "A_C_imag": _A_in["C_imag"],
        "A_log_dt": _A_in["log_dt"],
        "A_D_skip": _A_in["D_skip"],
        "B_q": _B_in["q"],
        "B_k": _B_in["k"],
        "B_v": _B_in["v"],
        "B_shift_k": _B_in["shift_k"],
        "B_ssm_k": _B_in["ssm_k"],
        "B_ssm_D": _B_in["ssm_D"],
        "gate_w": _w((lambda *s: torch.randn(*s, dtype=torch.bfloat16) * 0.05)(512) * 16.0),
    }

def reference(A_u, A_log_A_real, A_A_imag, A_C_real, A_C_imag, A_log_dt, A_D_skip, B_q, B_k, B_v, B_shift_k, B_ssm_k, B_ssm_D, gate_w):
    def _A_ref(u, log_A_real, A_imag, C_real, C_imag, log_dt, D_skip):
        L = u.shape[1]
        dt = torch.exp(log_dt)
        A = -torch.exp(log_A_real) + 1j * A_imag
        C = (C_real + 1j * C_imag) * dt[:, None]
        pos = (lambda x, dt: x.to(dt))(torch.arange(L), log_dt.dtype)
        P = torch.exp((A * dt[:, None])[:, :, None] * pos[None, None, :])
        K = 2.0 * torch.einsum("dn,dnl->dl", C, P).real
        L2 = 2 * L
        u_t = (lambda x, *d: x.permute(*d))(u, 0, 2, 1)
        Kf = (lambda x, *a, **k: torch.fft.rfft(x.float(), *a, **k))(K, L2)
        Uf = (lambda x, *a, **k: torch.fft.rfft(x.float(), *a, **k))(u_t, L2)
        y = (lambda x, *a, **k: torch.fft.irfft(x, *a, **k).to(torch.bfloat16))(Uf * Kf[None, :, :], L2)[:, :, :L]
        y = (lambda x, *d: x.permute(*d))(y, 0, 2, 1)
        return y + u * D_skip[None, None, :]

    def _B_ref(q, k, v, shift_k, ssm_k, ssm_D):
        B, S, D = q.shape
        ks = shift_k.shape[-1]
        k_pad = _pad(k, ((0, 0), (ks - 1, 0), (0, 0)))
        k_shift = torch.zeros_like(k)
        for t in range(ks):
            k_shift = k_shift + k_pad[:, t:t + S, :] * shift_k[:, ks - 1 - t][None, None, :]
        kv = k_shift * v
        L = 2 * S
        kv_pad = _pad(kv, ((0, 0), (0, S), (0, 0)))
        kf_pad = _pad(ssm_k, ((0, 0), (0, S)))
        kv_f = (lambda x, axes=(-2,-1): torch.fft.rfft2(x.float(), dim=axes))(kv_pad[:, None, :, :], axes=(1, 2))
        kf_f = (lambda x, axes=(-2,-1): torch.fft.rfft2(x.float(), dim=axes))(kf_pad[:, None, :], axes=(1, 2))
        kf_f = (lambda x, *d: x.permute(*d))(kf_f, 1, 2, 0)
        y_f = kv_f * kf_f[None, :, :, :]
        y = (lambda x, s, axes=(-2,-1): torch.fft.irfft2(x, s=s, dim=axes).to(torch.bfloat16))(y_f, (1, L), axes=(1, 2))[:, 0, :S, :]
        y = y + kv * ssm_D[None, None, :]
        return q * y

    x = A_u
    g = _sigmoid((x * gate_w).sum(-1, keepdims=True))
    a = _A_ref(x, A_log_A_real, A_A_imag, A_C_real, A_C_imag, A_log_dt, A_D_skip)
    b = _B_ref(x, B_k, B_v, B_shift_k, B_ssm_k, B_ssm_D)
    return g * a + (1 - g) * b
```

```cpp
#include <hip/hip_runtime.h>
#include <hip/hip_bf16.h>
#include <stdint.h>

typedef unsigned short u16;
__device__ __forceinline__ float bf2f(u16 x) { return __uint_as_float(((unsigned)x) << 16); }
__device__ __forceinline__ u16 f2bf(float f) {
    unsigned u = __float_as_uint(f);
    return (u16)((u + 0x7FFFu + ((u >> 16) & 1u)) >> 16);
}
__device__ __forceinline__ float rbf(float f) { return bf2f(f2bf(f)); }

constexpr int L = 2048, D = 512, N = 128;

__global__ void kgen(const u16* lar, const u16* aim, const u16* cre, const u16* cim, const u16* ldt, float* K) {
    int d = blockIdx.y; int l = blockIdx.x * 256 + threadIdx.x;
    float dt = rbf(expf(bf2f(ldt[d])));
    float pos = rbf((float)l);
    float acc = 0.f;
    for (int n = 0; n < N; ++n) {
        float zr = -rbf(expf(bf2f(lar[d * N + n]))) * dt;
        float zi = bf2f(aim[d * N + n]) * dt;
        float cr = bf2f(cre[d * N + n]) * dt, ci = bf2f(cim[d * N + n]) * dt;
        float e = expf(zr * pos), s, c; sincosf(zi * pos, &s, &c);
        acc += e * (cr * c - ci * s);
    }
    K[d * L + l] = 2.f * acc;
}
__global__ void pre(const u16* u, const u16* k, const u16* v, const u16* sk, const u16* gw, float* kv, float* g) {
    int t = blockIdx.x; int d = threadIdx.x;
    __shared__ float red[512];
    float ks = 0.f;
    for (int j = 3; j >= 0; --j) {
        float kk = (t - j >= 0) ? bf2f(k[(t - j) * D + d]) : 0.f;
        ks = rbf(ks + rbf(kk * bf2f(sk[d * 4 + j])));
    }
    kv[t * D + d] = rbf(ks * bf2f(v[t * D + d]));
    red[d] = rbf(bf2f(u[t * D + d]) * bf2f(gw[d]));
    __syncthreads();
    for (int s = 256; s > 0; s >>= 1) { if (d < s) red[d] += red[d + s]; __syncthreads(); }
    if (d == 0) { float x = rbf(red[0]); g[t] = rbf(1.f / (1.f + expf(-x))); }
}
__global__ void conv(const u16* u, const float* K, const float* kv, const u16* ssk, const u16* dsk, const u16* ssd, const float* g, u16* out) {
    int t = blockIdx.x; int d = threadIdx.x;
    float ya = 0.f, yb = 0.f;
    for (int s = 0; s <= t; ++s) {
        ya += K[d * L + (t - s)] * bf2f(u[s * D + d]);
        yb += bf2f(ssk[d * L + (t - s)]) * kv[s * D + d];
    }
    float uu = bf2f(u[t * D + d]);
    float a = rbf(rbf(ya) + rbf(uu * bf2f(dsk[d])));
    float y2 = rbf(rbf(yb) + rbf(kv[t * D + d] * bf2f(ssd[d])));
    float b = rbf(uu * y2);
    float gg = g[t];
    out[t * D + d] = f2bf(rbf(gg * a) + rbf(rbf(1.f - gg) * b));
}

extern "C" void kernel_launch(void* const* d_in, const int* in_sizes, int n_in,
                              void* d_out, int out_size, void* d_ws, size_t ws_size,
                              hipStream_t stream) {
    float* K = (float*)d_ws; float* kv = K + D * L; float* g = kv + D * L;
    kgen<<<dim3(L / 256, D), 256, 0, stream>>>((u16*)d_in[1], (u16*)d_in[2], (u16*)d_in[3], (u16*)d_in[4], (u16*)d_in[5], K);
    pre<<<L, 512, 0, stream>>>((u16*)d_in[0], (u16*)d_in[8], (u16*)d_in[9], (u16*)d_in[10], (u16*)d_in[13], kv, g);
    conv<<<L, 512, 0, stream>>>((u16*)d_in[0], K, kv, (u16*)d_in[11], (u16*)d_in[6], (u16*)d_in[12], g, (u16*)d_out);
}
```

```cpp
#include <hip/hip_runtime.h>
#include <stdint.h>

typedef unsigned short u16;
typedef unsigned int u32;
typedef unsigned long long u64;
typedef __attribute__((ext_vector_type(2))) __bf16 bf2;
typedef __attribute__((ext_vector_type(8))) __bf16 bf8;
typedef __attribute__((ext_vector_type(4))) short s4v;
typedef __attribute__((ext_vector_type(2))) u32 u2v;
typedef __attribute__((ext_vector_type(4))) u32 u4v;
typedef __attribute__((ext_vector_type(4))) float f4v;
typedef __attribute__((ext_vector_type(16))) float f16v;
#define GAS __attribute__((address_space(1)))
#define LAS __attribute__((address_space(3)))
typedef GAS u64 gu64;
#define RLX_AGENT __ATOMIC_RELAXED, __HIP_MEMORY_SCOPE_AGENT

#define STAMP(i)

constexpr int SEQ = 2048, DM = 512, NS = 128;

constexpr int XC_OFF = 0, XC_CONV = 8320, XC_COPY = 4160;
constexpr int Z_OFF = 33280, Z_CONV = 4608, Z_PAD = 512;
constexpr int Y_OFF = 51712;
constexpr int YC_OFF = Y_OFF + 32768;
constexpr int TAB_OFF = 92672, TAB_WAVE = 16384;
constexpr int FL_OFF = 158208;
constexpr int LDS_BYTES = 158272;
constexpr int TU_OFF = Y_OFF, TV_OFF = Y_OFF + 8192, TK_OFF = Y_OFF + 16384;
constexpr int WS_FLAG = 4096, WS_G = 32768, WS_X = 65536;
constexpr u32 SPIN_CAP = 1u << 20;

__device__ __forceinline__ float bf2f(u32 x) { return __uint_as_float(x << 16); }
__device__ __forceinline__ u32 pk2(float a, float b) { bf2 r; r[0] = (__bf16)a; r[1] = (__bf16)b; return __builtin_bit_cast(u32, r); }
__device__ __forceinline__ float rbf(float a) { return __uint_as_float(pk2(0.f, a) & 0xffff0000u); }
__device__ __forceinline__ float dot2(u32 a, u32 b, float c) {
    return __builtin_amdgcn_fdot2_f32_bf16(__builtin_bit_cast(bf2, a), __builtin_bit_cast(bf2, b), c, false);
}
struct cf { float r, i; };
__device__ __forceinline__ cf cmul(cf a, cf b) { return cf{a.r * b.r - a.i * b.i, a.r * b.i + a.i * b.r}; }
__device__ __forceinline__ cf cexp_p(float lz, float rev, float p) {
    float m = __builtin_amdgcn_exp2f(lz * p);
    float a = __builtin_amdgcn_fractf(rev * p);
    return cf{m * __builtin_amdgcn_cosf(a), m * __builtin_amdgcn_sinf(a)};
}
template <int CTRL> __device__ __forceinline__ float dppz(float v) {
    return __int_as_float(__builtin_amdgcn_update_dpp(0, __float_as_int(v), CTRL, 0xf, 0xf, true));
}

template <int MODE>
__global__ void __launch_bounds__(512, 2)
fused_k(const u16* __restrict__ gu, const u16* __restrict__ glar, const u16* __restrict__ gaim,
        const u16* __restrict__ gcre, const u16* __restrict__ gcim, const u16* __restrict__ gldt,
        const u16* __restrict__ gdsk, const u16* __restrict__ gk, const u16* __restrict__ gv,
        const u16* __restrict__ gshk, const u16* __restrict__ gssk, const u16* __restrict__ gssd,
        const u16* __restrict__ ggw, u16* __restrict__ gout, char* ws)
{
    __shared__ __attribute__((aligned(16))) char smem[LDS_BYTES];
    LAS char* sm = (LAS char*)smem;
    const int tid = threadIdx.x, l = tid & 63;
    const int w = __builtin_amdgcn_readfirstlane(tid >> 6);
    const int bx = blockIdx.x & 7, bj = blockIdx.x >> 3;
    const int d0 = 64 * bx + 2 * bj;
    STAMP(0);
    const u64 tag = (MODE == 0) ? __hip_atomic_load((gu64*)ws, RLX_AGENT) + 1ull : 1ull;
    const __amdgpu_buffer_rsrc_t xrs = __builtin_amdgcn_make_buffer_rsrc(ws + WS_X, 0, DM * 2 * 4096, 0x00020000);

    u2v shk; u4v grow0, grow1, gwv; u32 p_lar, p_aim, p_cre, p_cim, p_ldt;
    if (MODE != 2) {
        const int r = 8 * w + (l >> 3);
        const int f = ((r >> 1) & 1) | (((r >> 3) & 1) << 1);
        const size_t go = (size_t)(64 * bj + r) * DM + 64 * bx + 8 * ((l & 7) ^ (f << 1));
        __builtin_amdgcn_global_load_lds((const GAS u32*)(gk + go), (LAS u32*)(sm + TK_OFF + 1024 + 1024 * w), 16, 0, 0);
        __builtin_amdgcn_global_load_lds((const GAS u32*)(gv + go), (LAS u32*)(sm + TV_OFF + 1024 * w), 16, 0, 0);
        __builtin_amdgcn_global_load_lds((const GAS u32*)(gu + go), (LAS u32*)(sm + TU_OFF + 1024 * w), 16, 0, 0);
        if (w == 1) {
            if (bj > 0) __builtin_amdgcn_global_load_lds((const GAS u32*)(gk + go - 16 * DM), (LAS u32*)(sm + TK_OFF), 16, 0, 0);
            else *(LAS u4v*)(sm + TK_OFF + 16 * l) = u4v{0, 0, 0, 0};
        }
        shk = *(const u2v*)(gshk + 4 * (64 * bx + 16 * (w & 3) + (l & 15)));
    }
    if (MODE != 1) {
        __builtin_amdgcn_global_load_lds((const GAS u32*)(gssk + (size_t)(d0 + (w >> 2)) * SEQ + 512 * (w & 3) + 8 * l),
                                         (LAS u32*)(sm + Z_OFF + (2 + (w >> 2)) * Z_CONV + Z_PAD + 1024 * (w & 3)), 16, 0, 0);
    }
    if (w < 4) {
        if (MODE != 1) {
            const int o = (d0 + (w & 1)) * NS + 64 * (w >> 1) + l;
            p_lar = glar[o]; p_aim = gaim[o]; p_cre = gcre[o]; p_cim = gcim[o]; p_ldt = gldt[d0 + (w & 1)];
            if (l < 32) *(LAS u4v*)(sm + Z_OFF + w * Z_CONV + 16 * l) = u4v{0, 0, 0, 0};
            if (l < 2) *(LAS u4v*)(sm + XC_OFF + w * XC_CONV + l * XC_COPY + 4096) = u4v{0, 0, 0, 0};
            if (tid < 4) *(LAS u4v*)(sm + FL_OFF + 16 * tid) = u4v{0, 0, 0, 0};
        }
    } else if (MODE != 2) {
        const int row = 8 * blockIdx.x + 2 * (w - 4);
        grow0 = *(const u4v*)(gu + (size_t)row * DM + 8 * l);
        grow1 = *(const u4v*)(gu + (size_t)(row + 1) * DM + 8 * l);
        gwv = *(const u4v*)(ggw + 8 * l);
    }
    asm volatile("s_waitcnt vmcnt(0) lgkmcnt(0)" ::: "memory");
    __builtin_amdgcn_s_barrier();
    STAMP(1);

    if (MODE != 2) {
        const int cg = w & 3, th = w >> 2, G = l >> 4, q = (l >> 2) & 3, p = l & 3;
        const int t0 = 32 * th + 8 * G;
        const int row = t0 + 7 - q;
        const int f = ((row >> 1) & 1) | (((row >> 3) & 1) << 1);
        const int a0 = row * 128 + ((cg ^ f) << 5) + 8 * p;
        typedef LAS s4v* trp;
        s4v k0 = __builtin_amdgcn_ds_read_tr16_b64_v4i16((trp)(sm + TK_OFF + 1024 + a0));
        s4v k1 = __builtin_amdgcn_ds_read_tr16_b64_v4i16((trp)(sm + TK_OFF + 1024 + a0 - 512));
        s4v k2 = __builtin_amdgcn_ds_read_tr16_b64_v4i16((trp)(sm + TK_OFF + (a0 ^ 64)));
        s4v u0 = __builtin_amdgcn_ds_read_tr16_b64_v4i16((trp)(sm + TU_OFF + a0));
        s4v u1 = __builtin_amdgcn_ds_read_tr16_b64_v4i16((trp)(sm + TU_OFF + a0 - 512));
        s4v v0 = __builtin_amdgcn_ds_read_tr16_b64_v4i16((trp)(sm + TV_OFF + a0));
        s4v v1 = __builtin_amdgcn_ds_read_tr16_b64_v4i16((trp)(sm + TV_OFF + a0 - 512));
        const int ch = 64 * bx + 16 * cg + (l & 15);
        const u32 xo = (u32)ch * 8192u + 2u * (u32)(2040 - 64 * bj - t0);
        u2v ua = __builtin_bit_cast(u2v, u0), ub = __builtin_bit_cast(u2v, u1);
        __builtin_amdgcn_raw_buffer_store_b128(u4v{ua[0], ua[1], ub[0], ub[1]}, xrs, xo, 0, MODE == 0 ? 16 : 0);
        u2v ka = __builtin_bit_cast(u2v, k0), kb = __builtin_bit_cast(u2v, k1), kc = __builtin_bit_cast(u2v, k2);
        u2v va = __builtin_bit_cast(u2v, v0), vb = __builtin_bit_cast(u2v, v1);
        u32 K[6] = {ka[0], ka[1], kb[0], kb[1], kc[0], kc[1]};
        u32 V[4] = {va[0], va[1], vb[0], vb[1]};
        u32 Ks[5];
#pragma unroll
        for (int n = 0; n < 5; ++n) Ks[n] = __builtin_amdgcn_alignbit(K[n + 1], K[n], 16);
        u4v kv;
#pragma unroll
        for (int n = 0; n < 4; ++n) {
            float e0 = dot2(K[n], shk[0], dot2(K[n + 1], shk[1], 0.f));
            float e1 = dot2(Ks[n], shk[0], dot2(Ks[n + 1], shk[1], 0.f));
            kv[n] = pk2(e0 * bf2f(V[n] & 0xffffu), e1 * __uint_as_float(V[n] & 0xffff0000u));
        }
        __builtin_amdgcn_raw_buffer_store_b128(kv, xrs, xo + 4096u, 0, MODE == 0 ? 16 : 0);
        if (MODE == 0) {
            asm volatile("s_waitcnt vmcnt(0)" ::: "memory");
            if (l == 0) __hip_atomic_store((gu64*)(ws + WS_FLAG) + ((bx * 4 + cg) * 32 + bj) * 2 + th, tag, RLX_AGENT);
        }
    }
    STAMP(2);

    if (w < 4) {
        if (MODE != 1) {
        const float dt = rbf(expf(bf2f(p_ldt)));
        const float zr = -rbf(expf(bf2f(p_lar))) * dt;
        const float zi = bf2f(p_aim) * dt;
        const float lz = zr * 1.44269504088896341f, rev = zi * 0.15915494309189535f;
        const cf c2{2.f * (bf2f(p_cre) * dt), 2.f * (bf2f(p_cim) * dt)};
        const cf E1 = cexp_p(lz, rev, 1.f), E8 = cexp_p(lz, rev, 8.f), E64 = cexp_p(lz, rev, 64.f), E256 = cexp_p(lz, rev, 256.f);
        const cf E4 = cexp_p(lz, rev, 4.f);
        LAS char* tab = sm + TAB_OFF + w * TAB_WAVE;
        const int sw = (l >> 1) & 3;
        {
            cf e[8]; e[0] = cf{1.f, 0.f}; e[1] = E8;
#pragma unroll
            for (int g = 2; g < 8; ++g) e[g] = cmul(e[g - 1], E8);
#pragma unroll
            for (int cc = 0; cc < 4; ++cc) {
                if (cc > 0) {
#pragma unroll
                    for (int g = 0; g < 8; ++g) e[g] = cmul(e[g], E64);
                }
                u4v re{pk2(e[0].r, e[1].r), pk2(e[2].r, e[3].r), pk2(e[4].r, e[5].r), pk2(e[6].r, e[7].r)};
                u4v im{pk2(e[0].i, e[1].i), pk2(e[2].i, e[3].i), pk2(e[4].i, e[5].i), pk2(e[6].i, e[7].i)};
                *(LAS u4v*)(tab + 8192 + l * 64 + ((cc ^ sw) << 4)) = re;
                *(LAS u4v*)(tab + 8192 + 4096 + l * 64 + ((cc ^ sw) << 4)) = im;
            }
        }
        {
            cf a0[16], a1[16];
            a0[0] = c2;
#pragma unroll
            for (int g = 1; g < 8; ++g) a0[g] = cmul(a0[g - 1], E1);
            a0[8] = cmul(a0[0], E256); a0[9] = cmul(a0[2], E256); a0[10] = cmul(a0[4], E256); a0[11] = cmul(a0[6], E256);
            a0[12] = cmul(a0[8], E8);
            a0[13] = cmul(a0[8], E256); a0[14] = cmul(a0[13], E4); a0[15] = cmul(a0[13], E8);
            a1[0] = cmul(a0[13], E256); a1[1] = cmul(a1[0], E4); a1[2] = cmul(a1[0], E8);
            a1[3] = cmul(a1[0], E256); a1[4] = cmul(a1[3], E8);
            a1[5] = cmul(a1[3], E256); a1[6] = cmul(a1[5], E8);
            a1[7] = cmul(a1[5], E256); a1[8] = cmul(a1[7], E8);
            a1[9] = cmul(a1[7], E256); a1[10] = cmul(a1[9], E8);
#pragma unroll
            for (int g = 11; g < 16; ++g) a1[g] = cf{0.f, 0.f};
#pragma unroll
            for (int cc = 0; cc < 4; ++cc) {
                const int g = 4 * cc;
                u4v re{pk2(a0[g].r, a0[g + 1].r), pk2(a0[g + 2].r, a0[g + 3].r), pk2(a1[g].r, a1[g + 1].r), pk2(a1[g + 2].r, a1[g + 3].r)};
                u4v im{pk2(-a0[g].i, -a0[g + 1].i), pk2(-a0[g + 2].i, -a0[g + 3].i), pk2(-a1[g].i, -a1[g + 1].i), pk2(-a1[g + 2].i, -a1[g + 3].i)};
                *(LAS u4v*)(tab + l * 64 + ((cc ^ sw) << 4)) = re;
                *(LAS u4v*)(tab + 4096 + l * 64 + ((cc ^ sw) << 4)) = im;
            }
        }
        f16v kd = {};
        {
            const int h = l >> 5, blk = (l >> 4) & 1, q = (l >> 2) & 3, p = l & 3;
            typedef LAS s4v* trp;
            const int b0 = (8 * h + q) * 64 + (((2 * blk + (p >> 1)) ^ (q >> 1)) << 4) + 8 * (p & 1);
            const int b1 = (8 * h + 4 + q) * 64 + (((2 * blk + (p >> 1)) ^ (2 + (q >> 1))) << 4) + 8 * (p & 1);
#pragma unroll
            for (int s = 0; s < 8; ++s) {
                u2v x0 = __builtin_bit_cast(u2v, __builtin_amdgcn_ds_read_tr16_b64_v4i16((trp)(tab + s * 1024 + b0)));
                u2v x1 = __builtin_bit_cast(u2v, __builtin_amdgcn_ds_read_tr16_b64_v4i16((trp)(tab + s * 1024 + b1)));
                u2v y0 = __builtin_bit_cast(u2v, __builtin_amdgcn_ds_read_tr16_b64_v4i16((trp)(tab + 8192 + s * 1024 + b0)));
                u2v y1 = __builtin_bit_cast(u2v, __builtin_amdgcn_ds_read_tr16_b64_v4i16((trp)(tab + 8192 + s * 1024 + b1)));
                kd = __builtin_amdgcn_mfma_f32_32x32x16_bf16(__builtin_bit_cast(bf8, u4v{x0[0], x0[1], x1[0], x1[1]}),
                                                             __builtin_bit_cast(bf8, u4v{y0[0], y0[1], y1[0], y1[1]}), kd, 0, 0, 0);
            }
        }
        volatile LAS u32* pairf = (volatile LAS u32*)(sm + FL_OFF + 16 + 4 * (w & 1));
        if (w >= 2) {
#pragma unroll
            for (int g = 0; g < 4; ++g)
                *(LAS f4v*)(tab + g * 1024 + l * 16) = f4v{kd[4 * g], kd[4 * g + 1], kd[4 * g + 2], kd[4 * g + 3]};
            asm volatile("" ::: "memory");
            *pairf = 1u;
        } else {
            u32 spins = 0;
            while (*pairf == 0u && ++spins < SPIN_CAP) __builtin_amdgcn_s_sleep(1);
            asm volatile("" ::: "memory");
#pragma unroll
            for (int g = 0; g < 4; ++g) {
                f4v o = *(LAS f4v*)(tab + 2 * TAB_WAVE + g * 1024 + l * 16);
                kd[4 * g] += o[0]; kd[4 * g + 1] += o[1]; kd[4 * g + 2] += o[2]; kd[4 * g + 3] += o[3];
            }
            LAS char* zb = sm + Z_OFF + w * Z_CONV + Z_PAD + 16 * (l & 31);
            if (l < 32) {
                *(LAS u4v*)(zb) = u4v{pk2(kd[0], kd[1]), pk2(kd[2], kd[3]), pk2(kd[4], kd[5]), pk2(kd[6], kd[7])};
                *(LAS u4v*)(zb + 512) = u4v{pk2(kd[8], kd[8]), pk2(kd[9], kd[10]), pk2(kd[10], kd[10]), pk2(kd[11], kd[12])};
                *(LAS u4v*)(zb + 1024) = u4v{pk2(kd[13], kd[13]), pk2(kd[13], kd[14]), pk2(kd[14], kd[14]), pk2(kd[15], kd[15])};
            } else {
                *(LAS u4v*)(zb + 1536) = u4v{pk2(kd[0], kd[0]), pk2(kd[0], kd[1]), pk2(kd[1], kd[1]), pk2(kd[2], kd[2])};
#pragma unroll
                for (int a = 0; a < 4; ++a) {
                    const float lo = kd[3 + 2 * a], hi = kd[4 + 2 * a];
                    const u32 ll = pk2(lo, lo);
                    *(LAS u4v*)(zb + 2048 + 512 * a) = u4v{ll, ll, pk2((l & 1) ? hi : lo, hi), pk2(hi, hi)};
                }
            }
        }
        }
    } else {
        const int cw = w - 4;
        if (MODE != 2) {
            float s0 = 0.f, s1 = 0.f;
#pragma unroll
            for (int n = 0; n < 4; ++n) { s0 = dot2(grow0[n], gwv[n], s0); s1 = dot2(grow1[n], gwv[n], s1); }
#pragma unroll
            for (int o = 32; o > 0; o >>= 1) { s0 += __shfl_xor(s0, o); s1 += __shfl_xor(s1, o); }
            const float g0 = 1.f / (1.f + expf(-rbf(s0))), g1 = 1.f / (1.f + expf(-rbf(s1)));
            if (l == 0) __hip_atomic_store((gu64*)(ws + WS_G) + 4 * blockIdx.x + cw, (tag << 32) | pk2(g0, g1), RLX_AGENT);
        }
        if (MODE != 1) {
            if (MODE == 0) {
                gu64* fp = (gu64*)(ws + WS_FLAG) + (bx * 4 + (bj >> 3)) * 64 + l;
                u32 spins = 0;
                while (!__all(__hip_atomic_load(fp, RLX_AGENT) == tag) && ++spins < SPIN_CAP) __builtin_amdgcn_s_sleep(1);
                asm volatile("" ::: "memory");
            }
            STAMP(3);
            const u32 xo = (u32)(d0 + (cw & 1)) * 8192u + (u32)(cw >> 1) * 4096u + 16u * l;
            u4v xv[4];
#pragma unroll
            for (int pc = 3; pc >= 0; --pc) xv[pc] = __builtin_amdgcn_raw_buffer_load_b128(xrs, xo + 1024u * pc, 0, MODE == 0 ? 16 : 0);
            LAS char* xb = sm + XC_OFF + cw * XC_CONV + 16 * l;
#pragma unroll
            for (int pc = 3; pc >= 0; --pc) {
                const u32 nxt = (pc == 3) ? 0u : (u32)__builtin_amdgcn_readfirstlane(xv[pc == 3 ? 3 : pc + 1][0]);
                const u32 d4 = (u32)__builtin_amdgcn_update_dpp((int)nxt, (int)xv[pc][0], 0x130, 0xf, 0xf, false);
                *(LAS u4v*)(xb + 1024 * pc) = xv[pc];
                *(LAS u4v*)(xb + XC_COPY + 1024 * pc) =
                    u4v{__builtin_amdgcn_alignbit(xv[pc][1], xv[pc][0], 16), __builtin_amdgcn_alignbit(xv[pc][2], xv[pc][1], 16),
                        __builtin_amdgcn_alignbit(xv[pc][3], xv[pc][2], 16), __builtin_amdgcn_alignbit(d4, xv[pc][3], 16)};
            }
        }
    }
    if (MODE == 1) return;
    STAMP(4);
    asm volatile("s_waitcnt lgkmcnt(0)" ::: "memory");
    __builtin_amdgcn_s_barrier();
    STAMP(5);

    if (w < 4) {
        const LAS u32* ab = (const LAS u32*)(sm + XC_OFF + w * XC_CONV + ((l & 1) ? 0 : XC_COPY)) + (1023 - (l >> 1));
        const LAS char* bb = sm + Z_OFF + w * Z_CONV + Z_PAD + 16 * ((l & 31) - 4 * (l >> 5));
        LAS char* yb = sm + Y_OFF + w * 8192 + 16 * l;
        const u32 ycb = (u32)(size_t)(sm + YC_OFF + w * 2048 + 64 * (l >> 4));
        volatile LAS u32* done = (volatile LAS u32*)(sm + FL_OFF + 4 * w);
        constexpr int PD = 4, QRES = 8;
        u4v A[32], Br[12], Bs[8];
        f16v acc[2];
        auto ldA = [&](int i) { const LAS u32* p = ab - 32 * i; return u4v{p[0], p[1], p[2], p[3]}; };
        auto ldB = [&](int q) { return *(const LAS u4v*)(bb + 128 * q); };
        auto fetch = [&](int be, int i) {
            const int q = 4 * be - i;
            if (i >= 4 * be) A[i] = ldA(i);
            if (q > QRES) Bs[i & 7] = ldB(q);
            else if (i < 4) Br[q + 3] = ldB(q);
        };
        auto post = [&](int be, int ph) {
            f16v& a = acc[be & 1];
            if (ph < 3) {
#pragma unroll
                for (int e = 0; e < 4; ++e) a[4 * (2 - ph) + e] += dppz<0x111>(a[4 * (3 - ph) + e]);
            } else {
                *(LAS f4v*)(yb + 1024 * be) = f4v{a[0], a[1], a[2], a[3]};
                asm volatile("s_mov_b64 exec, %4\n\t"
                             "ds_write_b128 %0, %1 offset:%5\n\t"
                             "ds_write_b128 %0, %2 offset:%5+16\n\t"
                             "ds_write_b128 %0, %3 offset:%5+32\n\t"
                             "s_mov_b64 exec, -1"
                             :: "v"(ycb), "v"(f4v{a[4], a[5], a[6], a[7]}), "v"(f4v{a[8], a[9], a[10], a[11]}),
                                "v"(f4v{a[12], a[13], a[14], a[15]}), "s"(0x8000800080008000ull), "i"(256 * be) : "memory");
                *done = (u32)(be + 1);
            }
        };
#pragma unroll
        for (int i = 0; i < PD; ++i) fetch(0, i);
#pragma unroll
        for (int be = 0; be < 8; ++be) {
#pragma unroll
            for (int i = 0; i < 4 * be + 4; ++i) {
                const int q = 4 * be - i;
                const u4v bfr = (q > QRES) ? Bs[i & 7] : Br[q + 3];
                if (i == 0) acc[be & 1] = __builtin_amdgcn_mfma_f32_32x32x16_bf16(__builtin_bit_cast(bf8, A[i]), __builtin_bit_cast(bf8, bfr), f16v{}, 0, 0, 0);
                else acc[be & 1] = __builtin_amdgcn_mfma_f32_32x32x16_bf16(__builtin_bit_cast(bf8, A[i]), __builtin_bit_cast(bf8, bfr), acc[be & 1], 0, 0, 0);
                if (i + PD < 4 * be + 4) fetch(be, i + PD);
                else if (be < 7) fetch(be + 1, i + PD - 4 * be - 4);
                if (be > 0 && i >= 1 && i <= 4) post(be - 1, i - 1);
                __builtin_amdgcn_sched_barrier(0);
            }
        }
#pragma unroll
        for (int ph = 0; ph < 4; ++ph) post(7, ph);
        STAMP(6);
    } else {
        const int cw = w - 4;
        const int tl = 8 * (16 * (cw & 1) + (l >> 2)) + 4 * (cw >> 1) + (l & 3);
        float G[8], C1[8][2], C0[8][2];
        {
            gu64* gp = (gu64*)(ws + WS_G) + (tl >> 1);
            u32 gv2[8]; u32 spins = 0;
            for (;;) {
                bool ok = true;
#pragma unroll
                for (int be = 0; be < 8; ++be) {
                    const u64 xg = __hip_atomic_load(gp + 128 * be, RLX_AGENT);
                    gv2[be] = (u32)xg; ok &= ((u32)(xg >> 32) == (u32)tag);
                }
                if (__all(ok) || ++spins >= SPIN_CAP) break;
                __builtin_amdgcn_s_sleep(1);
            }
            if (MODE == 0 && blockIdx.x == 0) {
                if (l == 0) {
                    const u32 old = __hip_atomic_fetch_add((LAS u32*)(sm + FL_OFF + 32), 1u, __ATOMIC_RELAXED, __HIP_MEMORY_SCOPE_WORKGROUP);
                    if (old == 3u) __hip_atomic_store((gu64*)ws, tag, RLX_AGENT);
                }
            }
            const float ds0 = bf2f(gdsk[d0]), ds1 = bf2f(gdsk[d0 + 1]), sd0 = bf2f(gssd[d0]), sd1 = bf2f(gssd[d0 + 1]);
            const LAS u16* xr = (const LAS u16*)(sm + XC_OFF) + (2047 - tl);
#pragma unroll
            for (int be = 0; be < 8; ++be) {
                const float g = (tl & 1) ? __uint_as_float(gv2[be] & 0xffff0000u) : bf2f(gv2[be] & 0xffffu);
                const float og = rbf(1.f - g);
                G[be] = g;
#pragma unroll
                for (int c = 0; c < 2; ++c) {
                    const float uu = bf2f(xr[c * (XC_CONV / 2) - 256 * be]);
                    const float kv = bf2f(xr[(2 + c) * (XC_CONV / 2) - 256 * be]);
                    const float c1 = og * uu;
                    C1[be][c] = c1;
                    C0[be][c] = g * uu * (c ? ds1 : ds0) + c1 * kv * (c ? sd1 : sd0);
                }
            }
        }
        u32* op = (u32*)(gout + (size_t)tl * DM + d0);
        const LAS float* ym = (const LAS float*)(sm + Y_OFF) + 64 * cw + l;
        const LAS float* yc = (const LAS float*)(sm + YC_OFF) + ((cw & 1) ? 16 * (cw - 1) : 16 * (cw + 1) - 64) + (l < 12 ? l : 0);
        volatile LAS u4v* dn = (volatile LAS u4v*)(sm + FL_OFF);
#pragma unroll
        for (int be = 0; be < 8; ++be) {
            u32 spins = 0;
            for (;;) {
                const u4v d = *(const LAS u4v*)dn;
                asm volatile("" ::: "memory");
                const u32 m = min(min(d[0], d[1]), min(d[2], d[3]));
                if (__builtin_amdgcn_readfirstlane(m) > (u32)be || ++spins >= SPIN_CAP) break;
                __builtin_amdgcn_s_sleep(1);
            }
            float y[4];
#pragma unroll
            for (int cv = 0; cv < 4; ++cv) {
                y[cv] = ym[cv * 2048 + 256 * be];
                float cr = yc[cv * 512 + 64 * be];
                if (l >= 12 || (be == 0 && !(cw & 1))) cr = 0.f;
                y[cv] += cr;
            }
            const float o0 = G[be] * y[0] + (C1[be][0] * y[2] + C0[be][0]);
            const float o1 = G[be] * y[1] + (C1[be][1] * y[3] + C0[be][1]);
            op[(size_t)be * 256 * (DM / 2)] = pk2(o0, o1);
        }
        STAMP(6);
    }
}

#define KMODE 0
extern "C" void kernel_launch(void* const* d_in, const int* in_sizes, int n_in,
                              void* d_out, int out_size, void* d_ws, size_t ws_size,
                              hipStream_t stream) {
#define ARGS (const u16*)d_in[0], (const u16*)d_in[1], (const u16*)d_in[2], (const u16*)d_in[3], (const u16*)d_in[4], \
             (const u16*)d_in[5], (const u16*)d_in[6], (const u16*)d_in[8], (const u16*)d_in[9], (const u16*)d_in[10], \
             (const u16*)d_in[11], (const u16*)d_in[12], (const u16*)d_in[13], (u16*)d_out, (char*)d_ws
    fused_k<0><<<256, 512, 0, stream>>>(ARGS);
}
```

```cpp
#include <hip/hip_runtime.h>
#define STAMP(i) do { if ((threadIdx.x & 63) == 0) { unsigned long long t_ = __builtin_amdgcn_s_memrealtime(); \
    ((unsigned long long*)(ws + (16u << 20)))[(blockIdx.x * 8 + (threadIdx.x >> 6)) * 16 + (i)] = t_; } } while (0)
#define kernel_launch kernel_launch_inner
#include <hip/hip_runtime.h>
#include <stdint.h>

typedef unsigned short u16;
typedef unsigned int u32;
typedef unsigned long long u64;
typedef __attribute__((ext_vector_type(2))) __bf16 bf2;
typedef __attribute__((ext_vector_type(8))) __bf16 bf8;
typedef __attribute__((ext_vector_type(4))) short s4v;
typedef __attribute__((ext_vector_type(2))) u32 u2v;
typedef __attribute__((ext_vector_type(4))) u32 u4v;
typedef __attribute__((ext_vector_type(4))) float f4v;
typedef __attribute__((ext_vector_type(16))) float f16v;
#define GAS __attribute__((address_space(1)))
#define LAS __attribute__((address_space(3)))
typedef GAS u64 gu64;
#define RLX_AGENT __ATOMIC_RELAXED, __HIP_MEMORY_SCOPE_AGENT

constexpr int SEQ = 2048, DM = 512, NS = 128;

constexpr int XC_OFF = 0, XC_CONV = 8320, XC_COPY = 4160;
constexpr int Z_OFF = 33280, Z_CONV = 4608, Z_PAD = 512;
constexpr int Y_OFF = 51712;
constexpr int YC_OFF = Y_OFF + 32768;
constexpr int TAB_OFF = 92672, TAB_WAVE = 16384;
constexpr int FL_OFF = 158208;
constexpr int LDS_BYTES = 158272;
constexpr int TU_OFF = Y_OFF, TV_OFF = Y_OFF + 8192, TK_OFF = Y_OFF + 16384;
constexpr int WS_FLAG = 4096, WS_G = 32768, WS_X = 65536;
constexpr u32 SPIN_CAP = 1u << 20;

__device__ __forceinline__ float bf2f(u32 x) { return __uint_as_float(x << 16); }
__device__ __forceinline__ u32 pk2(float a, float b) { bf2 r; r[0] = (__bf16)a; r[1] = (__bf16)b; return __builtin_bit_cast(u32, r); }
__device__ __forceinline__ float rbf(float a) { return __uint_as_float(pk2(0.f, a) & 0xffff0000u); }
__device__ __forceinline__ float dot2(u32 a, u32 b, float c) {
    return __builtin_amdgcn_fdot2_f32_bf16(__builtin_bit_cast(bf2, a), __builtin_bit_cast(bf2, b), c, false);
}
struct cf { float r, i; };
__device__ __forceinline__ cf cmul(cf a, cf b) { return cf{a.r * b.r - a.i * b.i, a.r * b.i + a.i * b.r}; }
__device__ __forceinline__ cf cexp_p(float lz, float rev, float p) {
    float m = __builtin_amdgcn_exp2f(lz * p);
    float a = __builtin_amdgcn_fractf(rev * p);
    return cf{m * __builtin_amdgcn_cosf(a), m * __builtin_amdgcn_sinf(a)};
}
template <int CTRL> __device__ __forceinline__ float dppz(float v) {
    return __int_as_float(__builtin_amdgcn_update_dpp(0, __float_as_int(v), CTRL, 0xf, 0xf, true));
}

template <int MODE>
__global__ void __launch_bounds__(512, 2)
fused_k(const u16* __restrict__ gu, const u16* __restrict__ glar, const u16* __restrict__ gaim,
        const u16* __restrict__ gcre, const u16* __restrict__ gcim, const u16* __restrict__ gldt,
        const u16* __restrict__ gdsk, const u16* __restrict__ gk, const u16* __restrict__ gv,
        const u16* __restrict__ gshk, const u16* __restrict__ gssk, const u16* __restrict__ gssd,
        const u16* __restrict__ ggw, u16* __restrict__ gout, char* ws)
{
    __shared__ __attribute__((aligned(16))) char smem[LDS_BYTES];
    LAS char* sm = (LAS char*)smem;
    const int tid = threadIdx.x, l = tid & 63;
    const int w = __builtin_amdgcn_readfirstlane(tid >> 6);
    const int bx = blockIdx.x & 7, bj = blockIdx.x >> 3;
    const int d0 = 64 * bx + 2 * bj;
    STAMP(0);
    const u64 tag = (MODE == 0) ? __hip_atomic_load((gu64*)ws, RLX_AGENT) + 1ull : 1ull;
    const __amdgpu_buffer_rsrc_t xrs = __builtin_amdgcn_make_buffer_rsrc(ws + WS_X, 0, DM * 2 * 4096, 0x00020000);

    u2v shk; u4v grow0, grow1, gwv; u32 p_lar, p_aim, p_cre, p_cim, p_ldt;
    if (MODE != 2) {
        const int r = 8 * w + (l >> 3);
        const int f = ((r >> 1) & 1) | (((r >> 3) & 1) << 1);
        const size_t go = (size_t)(64 * bj + r) * DM + 64 * bx + 8 * ((l & 7) ^ (f << 1));
        __builtin_amdgcn_global_load_lds((const GAS u32*)(gk + go), (LAS u32*)(sm + TK_OFF + 1024 + 1024 * w), 16, 0, 0);
        __builtin_amdgcn_global_load_lds((const GAS u32*)(gv + go), (LAS u32*)(sm + TV_OFF + 1024 * w), 16, 0, 0);
        __builtin_amdgcn_global_load_lds((const GAS u32*)(gu + go), (LAS u32*)(sm + TU_OFF + 1024 * w), 16, 0, 0);
        if (w == 1) {
            if (bj > 0) __builtin_amdgcn_global_load_lds((const GAS u32*)(gk + go - 16 * DM), (LAS u32*)(sm + TK_OFF), 16, 0, 0);
            else *(LAS u4v*)(sm + TK_OFF + 16 * l) = u4v{0, 0, 0, 0};
        }
        shk = *(const u2v*)(gshk + 4 * (64 * bx + 16 * (w & 3) + (l & 15)));
    }
    if (MODE != 1) {
        __builtin_amdgcn_global_load_lds((const GAS u32*)(gssk + (size_t)(d0 + (w >> 2)) * SEQ + 512 * (w & 3) + 8 * l),
                                         (LAS u32*)(sm + Z_OFF + (2 + (w >> 2)) * Z_CONV + Z_PAD + 1024 * (w & 3)), 16, 0, 0);
    }
    if (w < 4) {
        if (MODE != 1) {
            const int o = (d0 + (w & 1)) * NS + 64 * (w >> 1) + l;
            p_lar = glar[o]; p_aim = gaim[o]; p_cre = gcre[o]; p_cim = gcim[o]; p_ldt = gldt[d0 + (w & 1)];
            if (l < 32) *(LAS u4v*)(sm + Z_OFF + w * Z_CONV + 16 * l) = u4v{0, 0, 0, 0};
            if (l < 2) *(LAS u4v*)(sm + XC_OFF + w * XC_CONV + l * XC_COPY + 4096) = u4v{0, 0, 0, 0};
            if (tid < 4) *(LAS u4v*)(sm + FL_OFF + 16 * tid) = u4v{0, 0, 0, 0};
        }
    } else if (MODE != 2) {
        const int row = 8 * blockIdx.x + 2 * (w - 4);
        grow0 = *(const u4v*)(gu + (size_t)row * DM + 8 * l);
        grow1 = *(const u4v*)(gu + (size_t)(row + 1) * DM + 8 * l);
        gwv = *(const u4v*)(ggw + 8 * l);
    }
    asm volatile("s_waitcnt vmcnt(0) lgkmcnt(0)" ::: "memory");
    __builtin_amdgcn_s_barrier();
    STAMP(1);

    if (MODE != 2) {
        const int cg = w & 3, th = w >> 2, G = l >> 4, q = (l >> 2) & 3, p = l & 3;
        const int t0 = 32 * th + 8 * G;
        const int row = t0 + 7 - q;
        const int f = ((row >> 1) & 1) | (((row >> 3) & 1) << 1);
        const int a0 = row * 128 + ((cg ^ f) << 5) + 8 * p;
        typedef LAS s4v* trp;
        s4v k0 = __builtin_amdgcn_ds_read_tr16_b64_v4i16((trp)(sm + TK_OFF + 1024 + a0));
        s4v k1 = __builtin_amdgcn_ds_read_tr16_b64_v4i16((trp)(sm + TK_OFF + 1024 + a0 - 512));
        s4v k2 = __builtin_amdgcn_ds_read_tr16_b64_v4i16((trp)(sm + TK_OFF + (a0 ^ 64)));
        s4v u0 = __builtin_amdgcn_ds_read_tr16_b64_v4i16((trp)(sm + TU_OFF + a0));
        s4v u1 = __builtin_amdgcn_ds_read_tr16_b64_v4i16((trp)(sm + TU_OFF + a0 - 512));
        s4v v0 = __builtin_amdgcn_ds_read_tr16_b64_v4i16((trp)(sm + TV_OFF + a0));
        s4v v1 = __builtin_amdgcn_ds_read_tr16_b64_v4i16((trp)(sm + TV_OFF + a0 - 512));
        const int ch = 64 * bx + 16 * cg + (l & 15);
        const u32 xo = (u32)ch * 8192u + 2u * (u32)(2040 - 64 * bj - t0);
        u2v ua = __builtin_bit_cast(u2v, u0), ub = __builtin_bit_cast(u2v, u1);
        __builtin_amdgcn_raw_buffer_store_b128(u4v{ua[0], ua[1], ub[0], ub[1]}, xrs, xo, 0, MODE == 0 ? 16 : 0);
        u2v ka = __builtin_bit_cast(u2v, k0), kb = __builtin_bit_cast(u2v, k1), kc = __builtin_bit_cast(u2v, k2);
        u2v va = __builtin_bit_cast(u2v, v0), vb = __builtin_bit_cast(u2v, v1);
        u32 K[6] = {ka[0], ka[1], kb[0], kb[1], kc[0], kc[1]};
        u32 V[4] = {va[0], va[1], vb[0], vb[1]};
        u32 Ks[5];
#pragma unroll
        for (int n = 0; n < 5; ++n) Ks[n] = __builtin_amdgcn_alignbit(K[n + 1], K[n], 16);
        u4v kv;
#pragma unroll
        for (int n = 0; n < 4; ++n) {
            float e0 = dot2(K[n], shk[0], dot2(K[n + 1], shk[1], 0.f));
            float e1 = dot2(Ks[n], shk[0], dot2(Ks[n + 1], shk[1], 0.f));
            kv[n] = pk2(e0 * bf2f(V[n] & 0xffffu), e1 * __uint_as_float(V[n] & 0xffff0000u));
        }
        __builtin_amdgcn_raw_buffer_store_b128(kv, xrs, xo + 4096u, 0, MODE == 0 ? 16 : 0);
        if (MODE == 0) {
            asm volatile("s_waitcnt vmcnt(0)" ::: "memory");
            if (l == 0) __hip_atomic_store((gu64*)(ws + WS_FLAG) + ((bx * 4 + cg) * 32 + bj) * 2 + th, tag, RLX_AGENT);
        }
    }
    STAMP(2);

    if (w < 4) {
        if (MODE != 1) {
        const float dt = rbf(expf(bf2f(p_ldt)));
        const float zr = -rbf(expf(bf2f(p_lar))) * dt;
        const float zi = bf2f(p_aim) * dt;
        const float lz = zr * 1.44269504088896341f, rev = zi * 0.15915494309189535f;
        const cf c2{2.f * (bf2f(p_cre) * dt), 2.f * (bf2f(p_cim) * dt)};
        const cf E1 = cexp_p(lz, rev, 1.f), E8 = cexp_p(lz, rev, 8.f), E64 = cexp_p(lz, rev, 64.f), E256 = cexp_p(lz, rev, 256.f);
        const cf E4 = cexp_p(lz, rev, 4.f);
        LAS char* tab = sm + TAB_OFF + w * TAB_WAVE;
        const int sw = (l >> 1) & 3;
        {
            cf e[8]; e[0] = cf{1.f, 0.f}; e[1] = E8;
#pragma unroll
            for (int g = 2; g < 8; ++g) e[g] = cmul(e[g - 1], E8);
#pragma unroll
            for (int cc = 0; cc < 4; ++cc) {
                if (cc > 0) {
#pragma unroll
                    for (int g = 0; g < 8; ++g) e[g] = cmul(e[g], E64);
                }
                u4v re{pk2(e[0].r, e[1].r), pk2(e[2].r, e[3].r), pk2(e[4].r, e[5].r), pk2(e[6].r, e[7].r)};
                u4v im{pk2(e[0].i, e[1].i), pk2(e[2].i, e[3].i), pk2(e[4].i, e[5].i), pk2(e[6].i, e[7].i)};
                *(LAS u4v*)(tab + 8192 + l * 64 + ((cc ^ sw) << 4)) = re;
                *(LAS u4v*)(tab + 8192 + 4096 + l * 64 + ((cc ^ sw) << 4)) = im;
            }
        }
        {
            cf a0[16], a1[16];
            a0[0] = c2;
#pragma unroll
            for (int g = 1; g < 8; ++g) a0[g] = cmul(a0[g - 1], E1);
            a0[8] = cmul(a0[0], E256); a0[9] = cmul(a0[2], E256); a0[10] = cmul(a0[4], E256); a0[11] = cmul(a0[6], E256);
            a0[12] = cmul(a0[8], E8);
            a0[13] = cmul(a0[8], E256); a0[14] = cmul(a0[13], E4); a0[15] = cmul(a0[13], E8);
            a1[0] = cmul(a0[13], E256); a1[1] = cmul(a1[0], E4); a1[2] = cmul(a1[0], E8);
            a1[3] = cmul(a1[0], E256); a1[4] = cmul(a1[3], E8);
            a1[5] = cmul(a1[3], E256); a1[6] = cmul(a1[5], E8);
            a1[7] = cmul(a1[5], E256); a1[8] = cmul(a1[7], E8);
            a1[9] = cmul(a1[7], E256); a1[10] = cmul(a1[9], E8);
#pragma unroll
            for (int g = 11; g < 16; ++g) a1[g] = cf{0.f, 0.f};
#pragma unroll
            for (int cc = 0; cc < 4; ++cc) {
                const int g = 4 * cc;
                u4v re{pk2(a0[g].r, a0[g + 1].r), pk2(a0[g + 2].r, a0[g + 3].r), pk2(a1[g].r, a1[g + 1].r), pk2(a1[g + 2].r, a1[g + 3].r)};
                u4v im{pk2(-a0[g].i, -a0[g + 1].i), pk2(-a0[g + 2].i, -a0[g + 3].i), pk2(-a1[g].i, -a1[g + 1].i), pk2(-a1[g + 2].i, -a1[g + 3].i)};
                *(LAS u4v*)(tab + l * 64 + ((cc ^ sw) << 4)) = re;
                *(LAS u4v*)(tab + 4096 + l * 64 + ((cc ^ sw) << 4)) = im;
            }
        }
        f16v kd = {};
        {
            const int h = l >> 5, blk = (l >> 4) & 1, q = (l >> 2) & 3, p = l & 3;
            typedef LAS s4v* trp;
            const int b0 = (8 * h + q) * 64 + (((2 * blk + (p >> 1)) ^ (q >> 1)) << 4) + 8 * (p & 1);
            const int b1 = (8 * h + 4 + q) * 64 + (((2 * blk + (p >> 1)) ^ (2 + (q >> 1))) << 4) + 8 * (p & 1);
#pragma unroll
            for (int s = 0; s < 8; ++s) {
                u2v x0 = __builtin_bit_cast(u2v, __builtin_amdgcn_ds_read_tr16_b64_v4i16((trp)(tab + s * 1024 + b0)));
                u2v x1 = __builtin_bit_cast(u2v, __builtin_amdgcn_ds_read_tr16_b64_v4i16((trp)(tab + s * 1024 + b1)));
                u2v y0 = __builtin_bit_cast(u2v, __builtin_amdgcn_ds_read_tr16_b64_v4i16((trp)(tab + 8192 + s * 1024 + b0)));
                u2v y1 = __builtin_bit_cast(u2v, __builtin_amdgcn_ds_read_tr16_b64_v4i16((trp)(tab + 8192 + s * 1024 + b1)));
                kd = __builtin_amdgcn_mfma_f32_32x32x16_bf16(__builtin_bit_cast(bf8, u4v{x0[0], x0[1], x1[0], x1[1]}),
                                                             __builtin_bit_cast(bf8, u4v{y0[0], y0[1], y1[0], y1[1]}), kd, 0, 0, 0);
            }
        }
        volatile LAS u32* pairf = (volatile LAS u32*)(sm + FL_OFF + 16 + 4 * (w & 1));
        if (w >= 2) {
#pragma unroll
            for (int g = 0; g < 4; ++g)
                *(LAS f4v*)(tab + g * 1024 + l * 16) = f4v{kd[4 * g], kd[4 * g + 1], kd[4 * g + 2], kd[4 * g + 3]};
            asm volatile("" ::: "memory");
            *pairf = 1u;
        } else {
            u32 spins = 0;
            while (*pairf == 0u && ++spins < SPIN_CAP) __builtin_amdgcn_s_sleep(1);
            asm volatile("" ::: "memory");
#pragma unroll
            for (int g = 0; g < 4; ++g) {
                f4v o = *(LAS f4v*)(tab + 2 * TAB_WAVE + g * 1024 + l * 16);
                kd[4 * g] += o[0]; kd[4 * g + 1] += o[1]; kd[4 * g + 2] += o[2]; kd[4 * g + 3] += o[3];
            }
            LAS char* zb = sm + Z_OFF + w * Z_CONV + Z_PAD + 16 * (l & 31);
            if (l < 32) {
                *(LAS u4v*)(zb) = u4v{pk2(kd[0], kd[1]), pk2(kd[2], kd[3]), pk2(kd[4], kd[5]), pk2(kd[6], kd[7])};
                *(LAS u4v*)(zb + 512) = u4v{pk2(kd[8], kd[8]), pk2(kd[9], kd[10]), pk2(kd[10], kd[10]), pk2(kd[11], kd[12])};
                *(LAS u4v*)(zb + 1024) = u4v{pk2(kd[13], kd[13]), pk2(kd[13], kd[14]), pk2(kd[14], kd[14]), pk2(kd[15], kd[15])};
            } else {
                *(LAS u4v*)(zb + 1536) = u4v{pk2(kd[0], kd[0]), pk2(kd[0], kd[1]), pk2(kd[1], kd[1]), pk2(kd[2], kd[2])};
#pragma unroll
                for (int a = 0; a < 4; ++a) {
                    const float lo = kd[3 + 2 * a], hi = kd[4 + 2 * a];
                    const u32 ll = pk2(lo, lo);
                    *(LAS u4v*)(zb + 2048 + 512 * a) = u4v{ll, ll, pk2((l & 1) ? hi : lo, hi), pk2(hi, hi)};
                }
            }
        }
        }
    } else {
        const int cw = w - 4;
        if (MODE != 2) {
            float s0 = 0.f, s1 = 0.f;
#pragma unroll
            for (int n = 0; n < 4; ++n) { s0 = dot2(grow0[n], gwv[n], s0); s1 = dot2(grow1[n], gwv[n], s1); }
#pragma unroll
            for (int o = 32; o > 0; o >>= 1) { s0 += __shfl_xor(s0, o); s1 += __shfl_xor(s1, o); }
            const float g0 = 1.f / (1.f + expf(-rbf(s0))), g1 = 1.f / (1.f + expf(-rbf(s1)));
            if (l == 0) __hip_atomic_store((gu64*)(ws + WS_G) + 4 * blockIdx.x + cw, (tag << 32) | pk2(g0, g1), RLX_AGENT);
        }
        if (MODE != 1) {
            if (MODE == 0) {
                gu64* fp = (gu64*)(ws + WS_FLAG) + (bx * 4 + (bj >> 3)) * 64 + l;
                u32 spins = 0;
                while (!__all(__hip_atomic_load(fp, RLX_AGENT) == tag) && ++spins < SPIN_CAP) __builtin_amdgcn_s_sleep(1);
                asm volatile("" ::: "memory");
            }
            STAMP(3);
            const u32 xo = (u32)(d0 + (cw & 1)) * 8192u + (u32)(cw >> 1) * 4096u + 16u * l;
            u4v xv[4];
#pragma unroll
            for (int pc = 3; pc >= 0; --pc) xv[pc] = __builtin_amdgcn_raw_buffer_load_b128(xrs, xo + 1024u * pc, 0, MODE == 0 ? 16 : 0);
            LAS char* xb = sm + XC_OFF + cw * XC_CONV + 16 * l;
#pragma unroll
            for (int pc = 3; pc >= 0; --pc) {
                const u32 nxt = (pc == 3) ? 0u : (u32)__builtin_amdgcn_readfirstlane(xv[pc == 3 ? 3 : pc + 1][0]);
                const u32 d4 = (u32)__builtin_amdgcn_update_dpp((int)nxt, (int)xv[pc][0], 0x130, 0xf, 0xf, false);
                *(LAS u4v*)(xb + 1024 * pc) = xv[pc];
                *(LAS u4v*)(xb + XC_COPY + 1024 * pc) =
                    u4v{__builtin_amdgcn_alignbit(xv[pc][1], xv[pc][0], 16), __builtin_amdgcn_alignbit(xv[pc][2], xv[pc][1], 16),
                        __builtin_amdgcn_alignbit(xv[pc][3], xv[pc][2], 16), __builtin_amdgcn_alignbit(d4, xv[pc][3], 16)};
            }
        }
    }
    if (MODE == 1) return;
    STAMP(4);
    asm volatile("s_waitcnt lgkmcnt(0)" ::: "memory");
    __builtin_amdgcn_s_barrier();
    STAMP(5);

    if (w < 4) {
        const LAS u32* ab = (const LAS u32*)(sm + XC_OFF + w * XC_CONV + ((l & 1) ? 0 : XC_COPY)) + (1023 - (l >> 1));
        const LAS char* bb = sm + Z_OFF + w * Z_CONV + Z_PAD + 16 * ((l & 31) - 4 * (l >> 5));
        LAS char* yb = sm + Y_OFF + w * 8192 + 16 * l;
        const u32 ycb = (u32)(size_t)(sm + YC_OFF + w * 2048 + 64 * (l >> 4));
        volatile LAS u32* done = (volatile LAS u32*)(sm + FL_OFF + 4 * w);
        constexpr int PD = 4, QRES = 8;
        u4v A[32], Br[12], Bs[8];
        f16v acc[2];
        auto ldA = [&](int i) { const LAS u32* p = ab - 32 * i; return u4v{p[0], p[1], p[2], p[3]}; };
        auto ldB = [&](int q) { return *(const LAS u4v*)(bb + 128 * q); };
        auto fetch = [&](int be, int i) {
            const int q = 4 * be - i;
            if (i >= 4 * be) A[i] = ldA(i);
            if (q > QRES) Bs[i & 7] = ldB(q);
            else if (i < 4) Br[q + 3] = ldB(q);
        };
        auto post = [&](int be, int ph) {
            f16v& a = acc[be & 1];
            if (ph < 3) {
#pragma unroll
                for (int e = 0; e < 4; ++e) a[4 * (2 - ph) + e] += dppz<0x111>(a[4 * (3 - ph) + e]);
            } else {
                *(LAS f4v*)(yb + 1024 * be) = f4v{a[0], a[1], a[2], a[3]};
                asm volatile("s_mov_b64 exec, %4\n\t"
                             "ds_write_b128 %0, %1 offset:%5\n\t"
                             "ds_write_b128 %0, %2 offset:%5+16\n\t"
                             "ds_write_b128 %0, %3 offset:%5+32\n\t"
                             "s_mov_b64 exec, -1"
                             :: "v"(ycb), "v"(f4v{a[4], a[5], a[6], a[7]}), "v"(f4v{a[8], a[9], a[10], a[11]}),
                                "v"(f4v{a[12], a[13], a[14], a[15]}), "s"(0x8000800080008000ull), "i"(256 * be) : "memory");
                *done = (u32)(be + 1);
            }
        };
#pragma unroll
        for (int i = 0; i < PD; ++i) fetch(0, i);
#pragma unroll
        for (int be = 0; be < 8; ++be) {
#pragma unroll
            for (int i = 0; i < 4 * be + 4; ++i) {
                const int q = 4 * be - i;
                const u4v bfr = (q > QRES) ? Bs[i & 7] : Br[q + 3];
                if (i == 0) acc[be & 1] = __builtin_amdgcn_mfma_f32_32x32x16_bf16(__builtin_bit_cast(bf8, A[i]), __builtin_bit_cast(bf8, bfr), f16v{}, 0, 0, 0);
                else acc[be & 1] = __builtin_amdgcn_mfma_f32_32x32x16_bf16(__builtin_bit_cast(bf8, A[i]), __builtin_bit_cast(bf8, bfr), acc[be & 1], 0, 0, 0);
                if (i + PD < 4 * be + 4) fetch(be, i + PD);
                else if (be < 7) fetch(be + 1, i + PD - 4 * be - 4);
                if (be > 0 && i >= 1 && i <= 4) post(be - 1, i - 1);
                __builtin_amdgcn_sched_barrier(0);
            }
        }
#pragma unroll
        for (int ph = 0; ph < 4; ++ph) post(7, ph);
        STAMP(6);
    } else {
        const int cw = w - 4;
        const int tl = 8 * (16 * (cw & 1) + (l >> 2)) + 4 * (cw >> 1) + (l & 3);
        float G[8], C1[8][2], C0[8][2];
        {
            gu64* gp = (gu64*)(ws + WS_G) + (tl >> 1);
            u32 gv2[8]; u32 spins = 0;
            for (;;) {
                bool ok = true;
#pragma unroll
                for (int be = 0; be < 8; ++be) {
                    const u64 xg = __hip_atomic_load(gp + 128 * be, RLX_AGENT);
                    gv2[be] = (u32)xg; ok &= ((u32)(xg >> 32) == (u32)tag);
                }
                if (__all(ok) || ++spins >= SPIN_CAP) break;
                __builtin_amdgcn_s_sleep(1);
            }
            if (MODE == 0 && blockIdx.x == 0) {
                if (l == 0) {
                    const u32 old = __hip_atomic_fetch_add((LAS u32*)(sm + FL_OFF + 32), 1u, __ATOMIC_RELAXED, __HIP_MEMORY_SCOPE_WORKGROUP);
                    if (old == 3u) __hip_atomic_store((gu64*)ws, tag, RLX_AGENT);
                }
            }
            const float ds0 = bf2f(gdsk[d0]), ds1 = bf2f(gdsk[d0 + 1]), sd0 = bf2f(gssd[d0]), sd1 = bf2f(gssd[d0 + 1]);
            const LAS u16* xr = (const LAS u16*)(sm + XC_OFF) + (2047 - tl);
#pragma unroll
            for (int be = 0; be < 8; ++be) {
                const float g = (tl & 1) ? __uint_as_float(gv2[be] & 0xffff0000u) : bf2f(gv2[be] & 0xffffu);
                const float og = rbf(1.f - g);
                G[be] = g;
#pragma unroll
                for (int c = 0; c < 2; ++c) {
                    const float uu = bf2f(xr[c * (XC_CONV / 2) - 256 * be]);
                    const float kv = bf2f(xr[(2 + c) * (XC_CONV / 2) - 256 * be]);
                    const float c1 = og * uu;
                    C1[be][c] = c1;
                    C0[be][c] = g * uu * (c ? ds1 : ds0) + c1 * kv * (c ? sd1 : sd0);
                }
            }
        }
        u32* op = (u32*)(gout + (size_t)tl * DM + d0);
        const LAS float* ym = (const LAS float*)(sm + Y_OFF) + 64 * cw + l;
        const LAS float* yc = (const LAS float*)(sm + YC_OFF) + ((cw & 1) ? 16 * (cw - 1) : 16 * (cw + 1) - 64) + (l < 12 ? l : 0);
        volatile LAS u4v* dn = (volatile LAS u4v*)(sm + FL_OFF);
#pragma unroll
        for (int be = 0; be < 8; ++be) {
            u32 spins = 0;
            for (;;) {
                const u4v d = *(const LAS u4v*)dn;
                asm volatile("" ::: "memory");
                const u32 m = min(min(d[0], d[1]), min(d[2], d[3]));
                if (__builtin_amdgcn_readfirstlane(m) > (u32)be || ++spins >= SPIN_CAP) break;
                __builtin_amdgcn_s_sleep(1);
            }
            float y[4];
#pragma unroll
            for (int cv = 0; cv < 4; ++cv) {
                y[cv] = ym[cv * 2048 + 256 * be];
                float cr = yc[cv * 512 + 64 * be];
                if (l >= 12 || (be == 0 && !(cw & 1))) cr = 0.f;
                y[cv] += cr;
            }
            const float o0 = G[be] * y[0] + (C1[be][0] * y[2] + C0[be][0]);
            const float o1 = G[be] * y[1] + (C1[be][1] * y[3] + C0[be][1]);
            op[(size_t)be * 256 * (DM / 2)] = pk2(o0, o1);
        }
        STAMP(6);
    }
}

#define KMODE 0
extern "C" void kernel_launch(void* const* d_in, const int* in_sizes, int n_in,
                              void* d_out, int out_size, void* d_ws, size_t ws_size,
                              hipStream_t stream) {
#define ARGS (const u16*)d_in[0], (const u16*)d_in[1], (const u16*)d_in[2], (const u16*)d_in[3], (const u16*)d_in[4], \
             (const u16*)d_in[5], (const u16*)d_in[6], (const u16*)d_in[8], (const u16*)d_in[9], (const u16*)d_in[10], \
             (const u16*)d_in[11], (const u16*)d_in[12], (const u16*)d_in[13], (u16*)d_out, (char*)d_ws
    fused_k<0><<<256, 512, 0, stream>>>(ARGS);
}

#define GRP 1
#define SIDX 0
#define STATMAX 1

#undef kernel_launch
__global__ void __launch_bounds__(256) diag_k(char* ws) {
    const unsigned long long* hs = (const unsigned long long*)(ws + (16u << 20));
    __shared__ unsigned long long smin[256], smax[256], ssum[256];
    int b = threadIdx.x; unsigned long long mn = ~0ull, mx = 0, sum = 0;
    for (int w = 0; w < 8; ++w) mn = min(mn, hs[(b * 8 + w) * 16]);
    for (int w = GRP * 4; w < GRP * 4 + 4; ++w) { unsigned long long x = hs[(b * 8 + w) * 16 + SIDX]; mx = max(mx, x); sum += x - mn; }
    smin[b] = mn; smax[b] = mx; ssum[b] = sum; __syncthreads();
    if (b == 0) {
        for (int i = 1; i < 256; ++i) { mn = min(mn, smin[i]); mx = max(mx, smax[i]); sum += ssum[i]; }
        unsigned long long v = STATMAX ? (mx - mn) : (sum / 1024);
        if (v > 4000) v = 4000;
        unsigned long long until = mn + 10000 + v * 20;
        while (__builtin_amdgcn_s_memrealtime() < until) __builtin_amdgcn_s_sleep(1);
    }
}
extern "C" void kernel_launch(void* const* d_in, const int* in_sizes, int n_in, void* d_out, int out_size, void* d_ws, size_t ws_size, hipStream_t stream) {
    kernel_launch_inner(d_in, in_sizes, n_in, d_out, out_size, d_ws, ws_size, stream);
    diag_k<<<1, 256, 0, stream>>>((char*)d_ws);
}
```

```cpp
#include <hip/hip_runtime.h>
#define STAMP(i) do { if ((threadIdx.x & 63) == 0) { unsigned long long t_ = __builtin_amdgcn_s_memrealtime(); \
    ((unsigned long long*)(ws + (16u << 20)))[(blockIdx.x * 8 + (threadIdx.x >> 6)) * 16 + (i)] = t_; } } while (0)
#define kernel_launch kernel_launch_inner
#include <hip/hip_runtime.h>
#include <stdint.h>

typedef unsigned short u16;
typedef unsigned int u32;
typedef unsigned long long u64;
typedef __attribute__((ext_vector_type(2))) __bf16 bf2;
typedef __attribute__((ext_vector_type(8))) __bf16 bf8;
typedef __attribute__((ext_vector_type(4))) short s4v;
typedef __attribute__((ext_vector_type(2))) u32 u2v;
typedef __attribute__((ext_vector_type(4))) u32 u4v;
typedef __attribute__((ext_vector_type(4))) float f4v;
typedef __attribute__((ext_vector_type(16))) float f16v;
#define GAS __attribute__((address_space(1)))
#define LAS __attribute__((address_space(3)))
typedef GAS u64 gu64;
#define RLX_AGENT __ATOMIC_RELAXED, __HIP_MEMORY_SCOPE_AGENT

constexpr int SEQ = 2048, DM = 512, NS = 128;

constexpr int XC_OFF = 0, XC_CONV = 8320, XC_COPY = 4160;
constexpr int Z_OFF = 33280, Z_CONV = 4608, Z_PAD = 512;
constexpr int Y_OFF = 51712;
constexpr int YC_OFF = Y_OFF + 32768;
constexpr int TAB_OFF = 92672, TAB_WAVE = 16384;
constexpr int FL_OFF = 158208;
constexpr int LDS_BYTES = 158272;
constexpr int TU_OFF = Y_OFF, TV_OFF = Y_OFF + 8192, TK_OFF = Y_OFF + 16384;
constexpr int WS_FLAG = 4096, WS_G = 32768, WS_X = 65536;
constexpr u32 SPIN_CAP = 1u << 20;

__device__ __forceinline__ float bf2f(u32 x) { return __uint_as_float(x << 16); }
__device__ __forceinline__ u32 pk2(float a, float b) { bf2 r; r[0] = (__bf16)a; r[1] = (__bf16)b; return __builtin_bit_cast(u32, r); }
__device__ __forceinline__ float rbf(float a) { return __uint_as_float(pk2(0.f, a) & 0xffff0000u); }
__device__ __forceinline__ float dot2(u32 a, u32 b, float c) {
    return __builtin_amdgcn_fdot2_f32_bf16(__builtin_bit_cast(bf2, a), __builtin_bit_cast(bf2, b), c, false);
}
struct cf { float r, i; };
__device__ __forceinline__ cf cmul(cf a, cf b) { return cf{a.r * b.r - a.i * b.i, a.r * b.i + a.i * b.r}; }
__device__ __forceinline__ cf cexp_p(float lz, float rev, float p) {
    float m = __builtin_amdgcn_exp2f(lz * p);
    float a = __builtin_amdgcn_fractf(rev * p);
    return cf{m * __builtin_amdgcn_cosf(a), m * __builtin_amdgcn_sinf(a)};
}
template <int CTRL> __device__ __forceinline__ float dppz(float v) {
    return __int_as_float(__builtin_amdgcn_update_dpp(0, __float_as_int(v), CTRL, 0xf, 0xf, true));
}

template <int MODE>
__global__ void __launch_bounds__(512, 2)
fused_k(const u16* __restrict__ gu, const u16* __restrict__ glar, const u16* __restrict__ gaim,
        const u16* __restrict__ gcre, const u16* __restrict__ gcim, const u16* __restrict__ gldt,
        const u16* __restrict__ gdsk, const u16* __restrict__ gk, const u16* __restrict__ gv,
        const u16* __restrict__ gshk, const u16* __restrict__ gssk, const u16* __restrict__ gssd,
        const u16* __restrict__ ggw, u16* __restrict__ gout, char* ws)
{
    __shared__ __attribute__((aligned(16))) char smem[LDS_BYTES];
    LAS char* sm = (LAS char*)smem;
    const int tid = threadIdx.x, l = tid & 63;
    const int w = __builtin_amdgcn_readfirstlane(tid >> 6);
    const int bx = blockIdx.x & 7, bj = blockIdx.x >> 3;
    const int d0 = 64 * bx + 2 * bj;
    STAMP(0);
    const u64 tag = (MODE == 0) ? __hip_atomic_load((gu64*)ws, RLX_AGENT) + 1ull : 1ull;
    const __amdgpu_buffer_rsrc_t xrs = __builtin_amdgcn_make_buffer_rsrc(ws + WS_X, 0, DM * 2 * 4096, 0x00020000);

    u2v shk; u4v grow0, grow1, gwv; u32 p_lar, p_aim, p_cre, p_cim, p_ldt;
    if (MODE != 2) {
        const int r = 8 * w + (l >> 3);
        const int f = ((r >> 1) & 1) | (((r >> 3) & 1) << 1);
        const size_t go = (size_t)(64 * bj + r) * DM + 64 * bx + 8 * ((l & 7) ^ (f << 1));
        __builtin_amdgcn_global_load_lds((const GAS u32*)(gk + go), (LAS u32*)(sm + TK_OFF + 1024 + 1024 * w), 16, 0, 0);
        __builtin_amdgcn_global_load_lds((const GAS u32*)(gv + go), (LAS u32*)(sm + TV_OFF + 1024 * w), 16, 0, 0);
        __builtin_amdgcn_global_load_lds((const GAS u32*)(gu + go), (LAS u32*)(sm + TU_OFF + 1024 * w), 16, 0, 0);
        if (w == 1) {
            if (bj > 0) __builtin_amdgcn_global_load_lds((const GAS u32*)(gk + go - 16 * DM), (LAS u32*)(sm + TK_OFF), 16, 0, 0);
            else *(LAS u4v*)(sm + TK_OFF + 16 * l) = u4v{0, 0, 0, 0};
        }
        shk = *(const u2v*)(gshk + 4 * (64 * bx + 16 * (w & 3) + (l & 15)));
    }
    if (MODE != 1) {
        __builtin_amdgcn_global_load_lds((const GAS u32*)(gssk + (size_t)(d0 + (w >> 2)) * SEQ + 512 * (w & 3) + 8 * l),
                                         (LAS u32*)(sm + Z_OFF + (2 + (w >> 2)) * Z_CONV + Z_PAD + 1024 * (w & 3)), 16, 0, 0);
    }
    if (w < 4) {
        if (MODE != 1) {
            const int o = (d0 + (w & 1)) * NS + 64 * (w >> 1) + l;
            p_lar = glar[o]; p_aim = gaim[o]; p_cre = gcre[o]; p_cim = gcim[o]; p_ldt = gldt[d0 + (w & 1)];
            if (l < 32) *(LAS u4v*)(sm + Z_OFF + w * Z_CONV + 16 * l) = u4v{0, 0, 0, 0};
            if (l < 2) *(LAS u4v*)(sm + XC_OFF + w * XC_CONV + l * XC_COPY + 4096) = u4v{0, 0, 0, 0};
            if (tid < 4) *(LAS u4v*)(sm + FL_OFF + 16 * tid) = u4v{0, 0, 0, 0};
        }
    } else if (MODE != 2) {
        const int row = 8 * blockIdx.x + 2 * (w - 4);
        grow0 = *(const u4v*)(gu + (size_t)row * DM + 8 * l);
        grow1 = *(const u4v*)(gu + (size_t)(row + 1) * DM + 8 * l);
        gwv = *(const u4v*)(ggw + 8 * l);
    }
    asm volatile("s_waitcnt vmcnt(0) lgkmcnt(0)" ::: "memory");
    __builtin_amdgcn_s_barrier();
    STAMP(1);

    if (MODE != 2) {
        const int cg = w & 3, th = w >> 2, G = l >> 4, q = (l >> 2) & 3, p = l & 3;
        const int t0 = 32 * th + 8 * G;
        const int row = t0 + 7 - q;
        const int f = ((row >> 1) & 1) | (((row >> 3) & 1) << 1);
        const int a0 = row * 128 + ((cg ^ f) << 5) + 8 * p;
        typedef LAS s4v* trp;
        s4v k0 = __builtin_amdgcn_ds_read_tr16_b64_v4i16((trp)(sm + TK_OFF + 1024 + a0));
        s4v k1 = __builtin_amdgcn_ds_read_tr16_b64_v4i16((trp)(sm + TK_OFF + 1024 + a0 - 512));
        s4v k2 = __builtin_amdgcn_ds_read_tr16_b64_v4i16((trp)(sm + TK_OFF + (a0 ^ 64)));
        s4v u0 = __builtin_amdgcn_ds_read_tr16_b64_v4i16((trp)(sm + TU_OFF + a0));
        s4v u1 = __builtin_amdgcn_ds_read_tr16_b64_v4i16((trp)(sm + TU_OFF + a0 - 512));
        s4v v0 = __builtin_amdgcn_ds_read_tr16_b64_v4i16((trp)(sm + TV_OFF + a0));
        s4v v1 = __builtin_amdgcn_ds_read_tr16_b64_v4i16((trp)(sm + TV_OFF + a0 - 512));
        const int ch = 64 * bx + 16 * cg + (l & 15);
        const u32 xo = (u32)ch * 8192u + 2u * (u32)(2040 - 64 * bj - t0);
        u2v ua = __builtin_bit_cast(u2v, u0), ub = __builtin_bit_cast(u2v, u1);
        __builtin_amdgcn_raw_buffer_store_b128(u4v{ua[0], ua[1], ub[0], ub[1]}, xrs, xo, 0, MODE == 0 ? 16 : 0);
        u2v ka = __builtin_bit_cast(u2v, k0), kb = __builtin_bit_cast(u2v, k1), kc = __builtin_bit_cast(u2v, k2);
        u2v va = __builtin_bit_cast(u2v, v0), vb = __builtin_bit_cast(u2v, v1);
        u32 K[6] = {ka[0], ka[1], kb[0], kb[1], kc[0], kc[1]};
        u32 V[4] = {va[0], va[1], vb[0], vb[1]};
        u32 Ks[5];
#pragma unroll
        for (int n = 0; n < 5; ++n) Ks[n] = __builtin_amdgcn_alignbit(K[n + 1], K[n], 16);
        u4v kv;
#pragma unroll
        for (int n = 0; n < 4; ++n) {
            float e0 = dot2(K[n], shk[0], dot2(K[n + 1], shk[1], 0.f));
            float e1 = dot2(Ks[n], shk[0], dot2(Ks[n + 1], shk[1], 0.f));
            kv[n] = pk2(e0 * bf2f(V[n] & 0xffffu), e1 * __uint_as_float(V[n] & 0xffff0000u));
        }
        __builtin_amdgcn_raw_buffer_store_b128(kv, xrs, xo + 4096u, 0, MODE == 0 ? 16 : 0);
        if (MODE == 0) {
            asm volatile("s_waitcnt vmcnt(0)" ::: "memory");
            if (l == 0) __hip_atomic_store((gu64*)(ws + WS_FLAG) + ((bx * 4 + cg) * 32 + bj) * 2 + th, tag, RLX_AGENT);
        }
    }
    STAMP(2);

    if (w < 4) {
        if (MODE != 1) {
        const float dt = rbf(expf(bf2f(p_ldt)));
        const float zr = -rbf(expf(bf2f(p_lar))) * dt;
        const float zi = bf2f(p_aim) * dt;
        const float lz = zr * 1.44269504088896341f, rev = zi * 0.15915494309189535f;
        const cf c2{2.f * (bf2f(p_cre) * dt), 2.f * (bf2f(p_cim) * dt)};
        const cf E1 = cexp_p(lz, rev, 1.f), E8 = cexp_p(lz, rev, 8.f), E64 = cexp_p(lz, rev, 64.f), E256 = cexp_p(lz, rev, 256.f);
        const cf E4 = cexp_p(lz, rev, 4.f);
        LAS char* tab = sm + TAB_OFF + w * TAB_WAVE;
        const int sw = (l >> 1) & 3;
        {
            cf e[8]; e[0] = cf{1.f, 0.f}; e[1] = E8;
#pragma unroll
            for (int g = 2; g < 8; ++g) e[g] = cmul(e[g - 1], E8);
#pragma unroll
            for (int cc = 0; cc < 4; ++cc) {
                if (cc > 0) {
#pragma unroll
                    for (int g = 0; g < 8; ++g) e[g] = cmul(e[g], E64);
                }
                u4v re{pk2(e[0].r, e[1].r), pk2(e[2].r, e[3].r), pk2(e[4].r, e[5].r), pk2(e[6].r, e[7].r)};
                u4v im{pk2(e[0].i, e[1].i), pk2(e[2].i, e[3].i), pk2(e[4].i, e[5].i), pk2(e[6].i, e[7].i)};
                *(LAS u4v*)(tab + 8192 + l * 64 + ((cc ^ sw) << 4)) = re;
                *(LAS u4v*)(tab + 8192 + 4096 + l * 64 + ((cc ^ sw) << 4)) = im;
            }
        }
        {
            cf a0[16], a1[16];
            a0[0] = c2;
#pragma unroll
            for (int g = 1; g < 8; ++g) a0[g] = cmul(a0[g - 1], E1);
            a0[8] = cmul(a0[0], E256); a0[9] = cmul(a0[2], E256); a0[10] = cmul(a0[4], E256); a0[11] = cmul(a0[6], E256);
            a0[12] = cmul(a0[8], E8);
            a0[13] = cmul(a0[8], E256); a0[14] = cmul(a0[13], E4); a0[15] = cmul(a0[13], E8);
            a1[0] = cmul(a0[13], E256); a1[1] = cmul(a1[0], E4); a1[2] = cmul(a1[0], E8);
            a1[3] = cmul(a1[0], E256); a1[4] = cmul(a1[3], E8);
            a1[5] = cmul(a1[3], E256); a1[6] = cmul(a1[5], E8);
            a1[7] = cmul(a1[5], E256); a1[8] = cmul(a1[7], E8);
            a1[9] = cmul(a1[7], E256); a1[10] = cmul(a1[9], E8);
#pragma unroll
            for (int g = 11; g < 16; ++g) a1[g] = cf{0.f, 0.f};
#pragma unroll
            for (int cc = 0; cc < 4; ++cc) {
                const int g = 4 * cc;
                u4v re{pk2(a0[g].r, a0[g + 1].r), pk2(a0[g + 2].r, a0[g + 3].r), pk2(a1[g].r, a1[g + 1].r), pk2(a1[g + 2].r, a1[g + 3].r)};
                u4v im{pk2(-a0[g].i, -a0[g + 1].i), pk2(-a0[g + 2].i, -a0[g + 3].i), pk2(-a1[g].i, -a1[g + 1].i), pk2(-a1[g + 2].i, -a1[g + 3].i)};
                *(LAS u4v*)(tab + l * 64 + ((cc ^ sw) << 4)) = re;
                *(LAS u4v*)(tab + 4096 + l * 64 + ((cc ^ sw) << 4)) = im;
            }
        }
        f16v kd = {};
        {
            const int h = l >> 5, blk = (l >> 4) & 1, q = (l >> 2) & 3, p = l & 3;
            typedef LAS s4v* trp;
            const int b0 = (8 * h + q) * 64 + (((2 * blk + (p >> 1)) ^ (q >> 1)) << 4) + 8 * (p & 1);
            const int b1 = (8 * h + 4 + q) * 64 + (((2 * blk + (p >> 1)) ^ (2 + (q >> 1))) << 4) + 8 * (p & 1);
#pragma unroll
            for (int s = 0; s < 8; ++s) {
                u2v x0 = __builtin_bit_cast(u2v, __builtin_amdgcn_ds_read_tr16_b64_v4i16((trp)(tab + s * 1024 + b0)));
                u2v x1 = __builtin_bit_cast(u2v, __builtin_amdgcn_ds_read_tr16_b64_v4i16((trp)(tab + s * 1024 + b1)));
                u2v y0 = __builtin_bit_cast(u2v, __builtin_amdgcn_ds_read_tr16_b64_v4i16((trp)(tab + 8192 + s * 1024 + b0)));
                u2v y1 = __builtin_bit_cast(u2v, __builtin_amdgcn_ds_read_tr16_b64_v4i16((trp)(tab + 8192 + s * 1024 + b1)));
                kd = __builtin_amdgcn_mfma_f32_32x32x16_bf16(__builtin_bit_cast(bf8, u4v{x0[0], x0[1], x1[0], x1[1]}),
                                                             __builtin_bit_cast(bf8, u4v{y0[0], y0[1], y1[0], y1[1]}), kd, 0, 0, 0);
            }
        }
        volatile LAS u32* pairf = (volatile LAS u32*)(sm + FL_OFF + 16 + 4 * (w & 1));
        if (w >= 2) {
#pragma unroll
            for (int g = 0; g < 4; ++g)
                *(LAS f4v*)(tab + g * 1024 + l * 16) = f4v{kd[4 * g], kd[4 * g + 1], kd[4 * g + 2], kd[4 * g + 3]};
            asm volatile("" ::: "memory");
            *pairf = 1u;
        } else {
            u32 spins = 0;
            while (*pairf == 0u && ++spins < SPIN_CAP) __builtin_amdgcn_s_sleep(1);
            asm volatile("" ::: "memory");
#pragma unroll
            for (int g = 0; g < 4; ++g) {
                f4v o = *(LAS f4v*)(tab + 2 * TAB_WAVE + g * 1024 + l * 16);
                kd[4 * g] += o[0]; kd[4 * g + 1] += o[1]; kd[4 * g + 2] += o[2]; kd[4 * g + 3] += o[3];
            }
            LAS char* zb = sm + Z_OFF + w * Z_CONV + Z_PAD + 16 * (l & 31);
            if (l < 32) {
                *(LAS u4v*)(zb) = u4v{pk2(kd[0], kd[1]), pk2(kd[2], kd[3]), pk2(kd[4], kd[5]), pk2(kd[6], kd[7])};
                *(LAS u4v*)(zb + 512) = u4v{pk2(kd[8], kd[8]), pk2(kd[9], kd[10]), pk2(kd[10], kd[10]), pk2(kd[11], kd[12])};
                *(LAS u4v*)(zb + 1024) = u4v{pk2(kd[13], kd[13]), pk2(kd[13], kd[14]), pk2(kd[14], kd[14]), pk2(kd[15], kd[15])};
            } else {
                *(LAS u4v*)(zb + 1536) = u4v{pk2(kd[0], kd[0]), pk2(kd[0], kd[1]), pk2(kd[1], kd[1]), pk2(kd[2], kd[2])};
#pragma unroll
                for (int a = 0; a < 4; ++a) {
                    const float lo = kd[3 + 2 * a], hi = kd[4 + 2 * a];
                    const u32 ll = pk2(lo, lo);
                    *(LAS u4v*)(zb + 2048 + 512 * a) = u4v{ll, ll, pk2((l & 1) ? hi : lo, hi), pk2(hi, hi)};
                }
            }
        }
        }
    } else {
        const int cw = w - 4;
        if (MODE != 2) {
            float s0 = 0.f, s1 = 0.f;
#pragma unroll
            for (int n = 0; n < 4; ++n) { s0 = dot2(grow0[n], gwv[n], s0); s1 = dot2(grow1[n], gwv[n], s1); }
#pragma unroll
            for (int o = 32; o > 0; o >>= 1) { s0 += __shfl_xor(s0, o); s1 += __shfl_xor(s1, o); }
            const float g0 = 1.f / (1.f + expf(-rbf(s0))), g1 = 1.f / (1.f + expf(-rbf(s1)));
            if (l == 0) __hip_atomic_store((gu64*)(ws + WS_G) + 4 * blockIdx.x + cw, (tag << 32) | pk2(g0, g1), RLX_AGENT);
        }
        if (MODE != 1) {
            if (MODE == 0) {
                gu64* fp = (gu64*)(ws + WS_FLAG) + (bx * 4 + (bj >> 3)) * 64 + l;
                u32 spins = 0;
                while (!__all(__hip_atomic_load(fp, RLX_AGENT) == tag) && ++spins < SPIN_CAP) __builtin_amdgcn_s_sleep(1);
                asm volatile("" ::: "memory");
            }
            STAMP(3);
            const u32 xo = (u32)(d0 + (cw & 1)) * 8192u + (u32)(cw >> 1) * 4096u + 16u * l;
            u4v xv[4];
#pragma unroll
            for (int pc = 3; pc >= 0; --pc) xv[pc] = __builtin_amdgcn_raw_buffer_load_b128(xrs, xo + 1024u * pc, 0, MODE == 0 ? 16 : 0);
            LAS char* xb = sm + XC_OFF + cw * XC_CONV + 16 * l;
#pragma unroll
            for (int pc = 3; pc >= 0; --pc) {
                const u32 nxt = (pc == 3) ? 0u : (u32)__builtin_amdgcn_readfirstlane(xv[pc == 3 ? 3 : pc + 1][0]);
                const u32 d4 = (u32)__builtin_amdgcn_update_dpp((int)nxt, (int)xv[pc][0], 0x130, 0xf, 0xf, false);
                *(LAS u4v*)(xb + 1024 * pc) = xv[pc];
                *(LAS u4v*)(xb + XC_COPY + 1024 * pc) =
                    u4v{__builtin_amdgcn_alignbit(xv[pc][1], xv[pc][0], 16), __builtin_amdgcn_alignbit(xv[pc][2], xv[pc][1], 16),
                        __builtin_amdgcn_alignbit(xv[pc][3], xv[pc][2], 16), __builtin_amdgcn_alignbit(d4, xv[pc][3], 16)};
            }
        }
    }
    if (MODE == 1) return;
    STAMP(4);
    asm volatile("s_waitcnt lgkmcnt(0)" ::: "memory");
    __builtin_amdgcn_s_barrier();
    STAMP(5);

    if (w < 4) {
        const LAS u32* ab = (const LAS u32*)(sm + XC_OFF + w * XC_CONV + ((l & 1) ? 0 : XC_COPY)) + (1023 - (l >> 1));
        const LAS char* bb = sm + Z_OFF + w * Z_CONV + Z_PAD + 16 * ((l & 31) - 4 * (l >> 5));
        LAS char* yb = sm + Y_OFF + w * 8192 + 16 * l;
        const u32 ycb = (u32)(size_t)(sm + YC_OFF + w * 2048 + 64 * (l >> 4));
        volatile LAS u32* done = (volatile LAS u32*)(sm + FL_OFF + 4 * w);
        constexpr int PD = 4, QRES = 8;
        u4v A[32], Br[12], Bs[8];
        f16v acc[2];
        auto ldA = [&](int i) { const LAS u32* p = ab - 32 * i; return u4v{p[0], p[1], p[2], p[3]}; };
        auto ldB = [&](int q) { return *(const LAS u4v*)(bb + 128 * q); };
        auto fetch = [&](int be, int i) {
            const int q = 4 * be - i;
            if (i >= 4 * be) A[i] = ldA(i);
            if (q > QRES) Bs[i & 7] = ldB(q);
            else if (i < 4) Br[q + 3] = ldB(q);
        };
        auto post = [&](int be, int ph) {
            f16v& a = acc[be & 1];
            if (ph < 3) {
#pragma unroll
                for (int e = 0; e < 4; ++e) a[4 * (2 - ph) + e] += dppz<0x111>(a[4 * (3 - ph) + e]);
            } else {
                *(LAS f4v*)(yb + 1024 * be) = f4v{a[0], a[1], a[2], a[3]};
                asm volatile("s_mov_b64 exec, %4\n\t"
                             "ds_write_b128 %0, %1 offset:%5\n\t"
                             "ds_write_b128 %0, %2 offset:%5+16\n\t"
                             "ds_write_b128 %0, %3 offset:%5+32\n\t"
                             "s_mov_b64 exec, -1"
                             :: "v"(ycb), "v"(f4v{a[4], a[5], a[6], a[7]}), "v"(f4v{a[8], a[9], a[10], a[11]}),
                                "v"(f4v{a[12], a[13], a[14], a[15]}), "s"(0x8000800080008000ull), "i"(256 * be) : "memory");
                *done = (u32)(be + 1);
            }
        };
#pragma unroll
        for (int i = 0; i < PD; ++i) fetch(0, i);
#pragma unroll
        for (int be = 0; be < 8; ++be) {
#pragma unroll
            for (int i = 0; i < 4 * be + 4; ++i) {
                const int q = 4 * be - i;
                const u4v bfr = (q > QRES) ? Bs[i & 7] : Br[q + 3];
                if (i == 0) acc[be & 1] = __builtin_amdgcn_mfma_f32_32x32x16_bf16(__builtin_bit_cast(bf8, A[i]), __builtin_bit_cast(bf8, bfr), f16v{}, 0, 0, 0);
                else acc[be & 1] = __builtin_amdgcn_mfma_f32_32x32x16_bf16(__builtin_bit_cast(bf8, A[i]), __builtin_bit_cast(bf8, bfr), acc[be & 1], 0, 0, 0);
                if (i + PD < 4 * be + 4) fetch(be, i + PD);
                else if (be < 7) fetch(be + 1, i + PD - 4 * be - 4);
                if (be > 0 && i >= 1 && i <= 4) post(be - 1, i - 1);
                __builtin_amdgcn_sched_barrier(0);
            }
        }
#pragma unroll
        for (int ph = 0; ph < 4; ++ph) post(7, ph);
        STAMP(6);
    } else {
        const int cw = w - 4;
        const int tl = 8 * (16 * (cw & 1) + (l >> 2)) + 4 * (cw >> 1) + (l & 3);
        float G[8], C1[8][2], C0[8][2];
        {
            gu64* gp = (gu64*)(ws + WS_G) + (tl >> 1);
            u32 gv2[8]; u32 spins = 0;
            for (;;) {
                bool ok = true;
#pragma unroll
                for (int be = 0; be < 8; ++be) {
                    const u64 xg = __hip_atomic_load(gp + 128 * be, RLX_AGENT);
                    gv2[be] = (u32)xg; ok &= ((u32)(xg >> 32) == (u32)tag);
                }
                if (__all(ok) || ++spins >= SPIN_CAP) break;
                __builtin_amdgcn_s_sleep(1);
            }
            if (MODE == 0 && blockIdx.x == 0) {
                if (l == 0) {
                    const u32 old = __hip_atomic_fetch_add((LAS u32*)(sm + FL_OFF + 32), 1u, __ATOMIC_RELAXED, __HIP_MEMORY_SCOPE_WORKGROUP);
                    if (old == 3u) __hip_atomic_store((gu64*)ws, tag, RLX_AGENT);
                }
            }
            const float ds0 = bf2f(gdsk[d0]), ds1 = bf2f(gdsk[d0 + 1]), sd0 = bf2f(gssd[d0]), sd1 = bf2f(gssd[d0 + 1]);
            const LAS u16* xr = (const LAS u16*)(sm + XC_OFF) + (2047 - tl);
#pragma unroll
            for (int be = 0; be < 8; ++be) {
                const float g = (tl & 1) ? __uint_as_float(gv2[be] & 0xffff0000u) : bf2f(gv2[be] & 0xffffu);
                const float og = rbf(1.f - g);
                G[be] = g;
#pragma unroll
                for (int c = 0; c < 2; ++c) {
                    const float uu = bf2f(xr[c * (XC_CONV / 2) - 256 * be]);
                    const float kv = bf2f(xr[(2 + c) * (XC_CONV / 2) - 256 * be]);
                    const float c1 = og * uu;
                    C1[be][c] = c1;
                    C0[be][c] = g * uu * (c ? ds1 : ds0) + c1 * kv * (c ? sd1 : sd0);
                }
            }
        }
        u32* op = (u32*)(gout + (size_t)tl * DM + d0);
        const LAS float* ym = (const LAS float*)(sm + Y_OFF) + 64 * cw + l;
        const LAS float* yc = (const LAS float*)(sm + YC_OFF) + ((cw & 1) ? 16 * (cw - 1) : 16 * (cw + 1) - 64) + (l < 12 ? l : 0);
        volatile LAS u4v* dn = (volatile LAS u4v*)(sm + FL_OFF);
#pragma unroll
        for (int be = 0; be < 8; ++be) {
            u32 spins = 0;
            for (;;) {
                const u4v d = *(const LAS u4v*)dn;
                asm volatile("" ::: "memory");
                const u32 m = min(min(d[0], d[1]), min(d[2], d[3]));
                if (__builtin_amdgcn_readfirstlane(m) > (u32)be || ++spins >= SPIN_CAP) break;
                __builtin_amdgcn_s_sleep(1);
            }
            float y[4];
#pragma unroll
            for (int cv = 0; cv < 4; ++cv) {
                y[cv] = ym[cv * 2048 + 256 * be];
                float cr = yc[cv * 512 + 64 * be];
                if (l >= 12 || (be == 0 && !(cw & 1))) cr = 0.f;
                y[cv] += cr;
            }
            const float o0 = G[be] * y[0] + (C1[be][0] * y[2] + C0[be][0]);
            const float o1 = G[be] * y[1] + (C1[be][1] * y[3] + C0[be][1]);
            op[(size_t)be * 256 * (DM / 2)] = pk2(o0, o1);
        }
        STAMP(6);
    }
}

#define KMODE 0
extern "C" void kernel_launch(void* const* d_in, const int* in_sizes, int n_in,
                              void* d_out, int out_size, void* d_ws, size_t ws_size,
                              hipStream_t stream) {
#define ARGS (const u16*)d_in[0], (const u16*)d_in[1], (const u16*)d_in[2], (const u16*)d_in[3], (const u16*)d_in[4], \
             (const u16*)d_in[5], (const u16*)d_in[6], (const u16*)d_in[8], (const u16*)d_in[9], (const u16*)d_in[10], \
             (const u16*)d_in[11], (const u16*)d_in[12], (const u16*)d_in[13], (u16*)d_out, (char*)d_ws
    fused_k<0><<<256, 512, 0, stream>>>(ARGS);
}

#define GRP 1
#define SIDX 6
#define STATMAX 2

#undef kernel_launch
__global__ void __launch_bounds__(256) diag_k(char* ws) {
    const unsigned long long* hs = (const unsigned long long*)(ws + (16u << 20));
    __shared__ unsigned long long smin[256], smax[256], ssum[256];
    unsigned long long tstart = __builtin_amdgcn_s_memrealtime();
    int b = threadIdx.x; unsigned long long mn = ~0ull, mx = 0, sum = 0;
    for (int w = 0; w < 8; ++w) mn = min(mn, hs[(b * 8 + w) * 16]);
    for (int w = GRP * 4; w < GRP * 4 + 4; ++w) { unsigned long long x = hs[(b * 8 + w) * 16 + SIDX]; mx = max(mx, x); sum += x - mn; }
    smin[b] = mn; smax[b] = mx; ssum[b] = sum; __syncthreads();
    if (b == 0) {
        for (int i = 1; i < 256; ++i) { mn = min(mn, smin[i]); mx = max(mx, smax[i]); sum += ssum[i]; }
        unsigned long long v = STATMAX == 1 ? (mx - mn) : STATMAX == 0 ? (sum / 1024) : STATMAX == 2 ? 0 : (tstart - mx);
        if (v > 4000) v = 4000;
        unsigned long long until = mn + 10000 + v * 20;
        while (__builtin_amdgcn_s_memrealtime() < until) __builtin_amdgcn_s_sleep(1);
    }
}
extern "C" void kernel_launch(void* const* d_in, const int* in_sizes, int n_in, void* d_out, int out_size, void* d_ws, size_t ws_size, hipStream_t stream) {
    kernel_launch_inner(d_in, in_sizes, n_in, d_out, out_size, d_ws, ws_size, stream);
    diag_k<<<1, 256, 0, stream>>>((char*)d_ws);
}
```

```cpp
#define BIGX 1
#define NB 256
#define WHAT 0

#include <hip/hip_runtime.h>
#define kernel_launch kernel_launch_inner
#include <hip/hip_runtime.h>
#include <stdint.h>

typedef unsigned short u16;
typedef unsigned int u32;
typedef unsigned long long u64;
typedef __attribute__((ext_vector_type(2))) __bf16 bf2;
typedef __attribute__((ext_vector_type(8))) __bf16 bf8;
typedef __attribute__((ext_vector_type(4))) short s4v;
typedef __attribute__((ext_vector_type(2))) u32 u2v;
typedef __attribute__((ext_vector_type(4))) u32 u4v;
typedef __attribute__((ext_vector_type(4))) float f4v;
typedef __attribute__((ext_vector_type(16))) float f16v;
#define GAS __attribute__((address_space(1)))
#define LAS __attribute__((address_space(3)))
typedef GAS u64 gu64;
#define RLX_AGENT __ATOMIC_RELAXED, __HIP_MEMORY_SCOPE_AGENT

#define STAMP(i)

constexpr int SEQ = 2048, DM = 512, NS = 128;

constexpr int XC_OFF = 0, XC_CONV = 8320, XC_COPY = 4160;
constexpr int Z_OFF = 33280, Z_CONV = 4608, Z_PAD = 512;
constexpr int Y_OFF = 51712;
constexpr int YC_OFF = Y_OFF + 32768;
constexpr int TAB_OFF = 92672, TAB_WAVE = 16384;
constexpr int FL_OFF = 158208;
constexpr int LDS_BYTES = 158272;
constexpr int TU_OFF = Y_OFF, TV_OFF = Y_OFF + 8192, TK_OFF = Y_OFF + 16384;
constexpr int WS_FLAG = 4096, WS_G = 32768, WS_X = 65536;
constexpr u32 SPIN_CAP = 1u << 20;

__device__ __forceinline__ float bf2f(u32 x) { return __uint_as_float(x << 16); }
__device__ __forceinline__ u32 pk2(float a, float b) { bf2 r; r[0] = (__bf16)a; r[1] = (__bf16)b; return __builtin_bit_cast(u32, r); }
__device__ __forceinline__ float rbf(float a) { return __uint_as_float(pk2(0.f, a) & 0xffff0000u); }
__device__ __forceinline__ float dot2(u32 a, u32 b, float c) {
    return __builtin_amdgcn_fdot2_f32_bf16(__builtin_bit_cast(bf2, a), __builtin_bit_cast(bf2, b), c, false);
}
struct cf { float r, i; };
__device__ __forceinline__ cf cmul(cf a, cf b) { return cf{a.r * b.r - a.i * b.i, a.r * b.i + a.i * b.r}; }
__device__ __forceinline__ cf cexp_p(float lz, float rev, float p) {
    float m = __builtin_amdgcn_exp2f(lz * p);
    float a = __builtin_amdgcn_fractf(rev * p);
    return cf{m * __builtin_amdgcn_cosf(a), m * __builtin_amdgcn_sinf(a)};
}
template <int CTRL> __device__ __forceinline__ float dppz(float v) {
    return __int_as_float(__builtin_amdgcn_update_dpp(0, __float_as_int(v), CTRL, 0xf, 0xf, true));
}

template <int MODE>
__global__ void __launch_bounds__(512, 2)
fused_k(const u16* __restrict__ gu, const u16* __restrict__ glar, const u16* __restrict__ gaim,
        const u16* __restrict__ gcre, const u16* __restrict__ gcim, const u16* __restrict__ gldt,
        const u16* __restrict__ gdsk, const u16* __restrict__ gk, const u16* __restrict__ gv,
        const u16* __restrict__ gshk, const u16* __restrict__ gssk, const u16* __restrict__ gssd,
        const u16* __restrict__ ggw, u16* __restrict__ gout, char* ws)
{
    __shared__ __attribute__((aligned(16))) char smem[LDS_BYTES];
    LAS char* sm = (LAS char*)smem;
    const int tid = threadIdx.x, l = tid & 63;
    const int w = __builtin_amdgcn_readfirstlane(tid >> 6);
    const int bx = blockIdx.x & 7, bj = blockIdx.x >> 3;
    const int d0 = 64 * bx + 2 * bj;
    STAMP(0);
    const u64 tag = (MODE == 0) ? __hip_atomic_load((gu64*)ws, RLX_AGENT) + 1ull : 1ull;
    const __amdgpu_buffer_rsrc_t xrs = __builtin_amdgcn_make_buffer_rsrc(ws + WS_X, 0, DM * 2 * 4096, 0x00020000);

    u2v shk; u4v grow0, grow1, gwv; u32 p_lar, p_aim, p_cre, p_cim, p_ldt;
    if (MODE != 2) {
        const int r = 8 * w + (l >> 3);
        const int f = ((r >> 1) & 1) | (((r >> 3) & 1) << 1);
        const size_t go = (size_t)(64 * bj + r) * DM + 64 * bx + 8 * ((l & 7) ^ (f << 1));
        __builtin_amdgcn_global_load_lds((const GAS u32*)(gk + go), (LAS u32*)(sm + TK_OFF + 1024 + 1024 * w), 16, 0, 0);
        __builtin_amdgcn_global_load_lds((const GAS u32*)(gv + go), (LAS u32*)(sm + TV_OFF + 1024 * w), 16, 0, 0);
        __builtin_amdgcn_global_load_lds((const GAS u32*)(gu + go), (LAS u32*)(sm + TU_OFF + 1024 * w), 16, 0, 0);
        if (w == 1) {
            if (bj > 0) __builtin_amdgcn_global_load_lds((const GAS u32*)(gk + go - 16 * DM), (LAS u32*)(sm + TK_OFF), 16, 0, 0);
            else *(LAS u4v*)(sm + TK_OFF + 16 * l) = u4v{0, 0, 0, 0};
        }
        shk = *(const u2v*)(gshk + 4 * (64 * bx + 16 * (w & 3) + (l & 15)));
    }
    if (MODE != 1) {
        __builtin_amdgcn_global_load_lds((const GAS u32*)(gssk + (size_t)(d0 + (w >> 2)) * SEQ + 512 * (w & 3) + 8 * l),
                                         (LAS u32*)(sm + Z_OFF + (2 + (w >> 2)) * Z_CONV + Z_PAD + 1024 * (w & 3)), 16, 0, 0);
    }
    if (w < 4) {
        if (MODE != 1) {
            const int o = (d0 + (w & 1)) * NS + 64 * (w >> 1) + l;
            p_lar = glar[o]; p_aim = gaim[o]; p_cre = gcre[o]; p_cim = gcim[o]; p_ldt = gldt[d0 + (w & 1)];
            if (l < 32) *(LAS u4v*)(sm + Z_OFF + w * Z_CONV + 16 * l) = u4v{0, 0, 0, 0};
            if (l < 2) *(LAS u4v*)(sm + XC_OFF + w * XC_CONV + l * XC_COPY + 4096) = u4v{0, 0, 0, 0};
            if (tid < 4) *(LAS u4v*)(sm + FL_OFF + 16 * tid) = u4v{0, 0, 0, 0};
        }
    } else if (MODE != 2) {
        const int row = 8 * blockIdx.x + 2 * (w - 4);
        grow0 = *(const u4v*)(gu + (size_t)row * DM + 8 * l);
        grow1 = *(const u4v*)(gu + (size_t)(row + 1) * DM + 8 * l);
        gwv = *(const u4v*)(ggw + 8 * l);
    }
    asm volatile("s_waitcnt vmcnt(0) lgkmcnt(0)" ::: "memory");
    __builtin_amdgcn_s_barrier();
    STAMP(1);

    if (MODE != 2) {
        const int cg = w & 3, th = w >> 2, G = l >> 4, q = (l >> 2) & 3, p = l & 3;
        const int t0 = 32 * th + 8 * G;
        const int row = t0 + 7 - q;
        const int f = ((row >> 1) & 1) | (((row >> 3) & 1) << 1);
        const int a0 = row * 128 + ((cg ^ f) << 5) + 8 * p;
        typedef LAS s4v* trp;
        s4v k0 = __builtin_amdgcn_ds_read_tr16_b64_v4i16((trp)(sm + TK_OFF + 1024 + a0));
        s4v k1 = __builtin_amdgcn_ds_read_tr16_b64_v4i16((trp)(sm + TK_OFF + 1024 + a0 - 512));
        s4v k2 = __builtin_amdgcn_ds_read_tr16_b64_v4i16((trp)(sm + TK_OFF + (a0 ^ 64)));
        s4v u0 = __builtin_amdgcn_ds_read_tr16_b64_v4i16((trp)(sm + TU_OFF + a0));
        s4v u1 = __builtin_amdgcn_ds_read_tr16_b64_v4i16((trp)(sm + TU_OFF + a0 - 512));
        s4v v0 = __builtin_amdgcn_ds_read_tr16_b64_v4i16((trp)(sm + TV_OFF + a0));
        s4v v1 = __builtin_amdgcn_ds_read_tr16_b64_v4i16((trp)(sm + TV_OFF + a0 - 512));
        const int ch = 64 * bx + 16 * cg + (l & 15);
        const u32 xo = (u32)ch * 8192u + 2u * (u32)(2040 - 64 * bj - t0);
        u2v ua = __builtin_bit_cast(u2v, u0), ub = __builtin_bit_cast(u2v, u1);
        __builtin_amdgcn_raw_buffer_store_b128(u4v{ua[0], ua[1], ub[0], ub[1]}, xrs, xo, 0, MODE == 0 ? 16 : 0);
        u2v ka = __builtin_bit_cast(u2v, k0), kb = __builtin_bit_cast(u2v, k1), kc = __builtin_bit_cast(u2v, k2);
        u2v va = __builtin_bit_cast(u2v, v0), vb = __builtin_bit_cast(u2v, v1);
        u32 K[6] = {ka[0], ka[1], kb[0], kb[1], kc[0], kc[1]};
        u32 V[4] = {va[0], va[1], vb[0], vb[1]};
        u32 Ks[5];
#pragma unroll
        for (int n = 0; n < 5; ++n) Ks[n] = __builtin_amdgcn_alignbit(K[n + 1], K[n], 16);
        u4v kv;
#pragma unroll
        for (int n = 0; n < 4; ++n) {
            float e0 = dot2(K[n], shk[0], dot2(K[n + 1], shk[1], 0.f));
            float e1 = dot2(Ks[n], shk[0], dot2(Ks[n + 1], shk[1], 0.f));
            kv[n] = pk2(e0 * bf2f(V[n] & 0xffffu), e1 * __uint_as_float(V[n] & 0xffff0000u));
        }
        __builtin_amdgcn_raw_buffer_store_b128(kv, xrs, xo + 4096u, 0, MODE == 0 ? 16 : 0);
        if (MODE == 0) {
            asm volatile("s_waitcnt vmcnt(0)" ::: "memory");
            if (l == 0) __hip_atomic_store((gu64*)(ws + WS_FLAG) + ((bx * 4 + cg) * 32 + bj) * 2 + th, tag, RLX_AGENT);
        }
    }
    STAMP(2);

    if (w < 4) {
        if (MODE != 1) {
        const float dt = rbf(expf(bf2f(p_ldt)));
        const float zr = -rbf(expf(bf2f(p_lar))) * dt;
        const float zi = bf2f(p_aim) * dt;
        const float lz = zr * 1.44269504088896341f, rev = zi * 0.15915494309189535f;
        const cf c2{2.f * (bf2f(p_cre) * dt), 2.f * (bf2f(p_cim) * dt)};
        const cf E1 = cexp_p(lz, rev, 1.f), E8 = cexp_p(lz, rev, 8.f), E64 = cexp_p(lz, rev, 64.f), E256 = cexp_p(lz, rev, 256.f);
        const cf E4 = cexp_p(lz, rev, 4.f);
        LAS char* tab = sm + TAB_OFF + w * TAB_WAVE;
        const int sw = (l >> 1) & 3;
        {
            cf e[8]; e[0] = cf{1.f, 0.f}; e[1] = E8;
#pragma unroll
            for (int g = 2; g < 8; ++g) e[g] = cmul(e[g - 1], E8);
#pragma unroll
            for (int cc = 0; cc < 4; ++cc) {
                if (cc > 0) {
#pragma unroll
                    for (int g = 0; g < 8; ++g) e[g] = cmul(e[g], E64);
                }
                u4v re{pk2(e[0].r, e[1].r), pk2(e[2].r, e[3].r), pk2(e[4].r, e[5].r), pk2(e[6].r, e[7].r)};
                u4v im{pk2(e[0].i, e[1].i), pk2(e[2].i, e[3].i), pk2(e[4].i, e[5].i), pk2(e[6].i, e[7].i)};
                *(LAS u4v*)(tab + 8192 + l * 64 + ((cc ^ sw) << 4)) = re;
                *(LAS u4v*)(tab + 8192 + 4096 + l * 64 + ((cc ^ sw) << 4)) = im;
            }
        }
        {
            cf a0[16], a1[16];
            a0[0] = c2;
#pragma unroll
            for (int g = 1; g < 8; ++g) a0[g] = cmul(a0[g - 1], E1);
            a0[8] = cmul(a0[0], E256); a0[9] = cmul(a0[2], E256); a0[10] = cmul(a0[4], E256); a0[11] = cmul(a0[6], E256);
            a0[12] = cmul(a0[8], E8);
            a0[13] = cmul(a0[8], E256); a0[14] = cmul(a0[13], E4); a0[15] = cmul(a0[13], E8);
            a1[0] = cmul(a0[13], E256); a1[1] = cmul(a1[0], E4); a1[2] = cmul(a1[0], E8);
            a1[3] = cmul(a1[0], E256); a1[4] = cmul(a1[3], E8);
            a1[5] = cmul(a1[3], E256); a1[6] = cmul(a1[5], E8);
            a1[7] = cmul(a1[5], E256); a1[8] = cmul(a1[7], E8);
            a1[9] = cmul(a1[7], E256); a1[10] = cmul(a1[9], E8);
#pragma unroll
            for (int g = 11; g < 16; ++g) a1[g] = cf{0.f, 0.f};
#pragma unroll
            for (int cc = 0; cc < 4; ++cc) {
                const int g = 4 * cc;
                u4v re{pk2(a0[g].r, a0[g + 1].r), pk2(a0[g + 2].r, a0[g + 3].r), pk2(a1[g].r, a1[g + 1].r), pk2(a1[g + 2].r, a1[g + 3].r)};
                u4v im{pk2(-a0[g].i, -a0[g + 1].i), pk2(-a0[g + 2].i, -a0[g + 3].i), pk2(-a1[g].i, -a1[g + 1].i), pk2(-a1[g + 2].i, -a1[g + 3].i)};
                *(LAS u4v*)(tab + l * 64 + ((cc ^ sw) << 4)) = re;
                *(LAS u4v*)(tab + 4096 + l * 64 + ((cc ^ sw) << 4)) = im;
            }
        }
        f16v kd = {};
        {
            const int h = l >> 5, blk = (l >> 4) & 1, q = (l >> 2) & 3, p = l & 3;
            typedef LAS s4v* trp;
            const int b0 = (8 * h + q) * 64 + (((2 * blk + (p >> 1)) ^ (q >> 1)) << 4) + 8 * (p & 1);
            const int b1 = (8 * h + 4 + q) * 64 + (((2 * blk + (p >> 1)) ^ (2 + (q >> 1))) << 4) + 8 * (p & 1);
#pragma unroll
            for (int s = 0; s < 8; ++s) {
                u2v x0 = __builtin_bit_cast(u2v, __builtin_amdgcn_ds_read_tr16_b64_v4i16((trp)(tab + s * 1024 + b0)));
                u2v x1 = __builtin_bit_cast(u2v, __builtin_amdgcn_ds_read_tr16_b64_v4i16((trp)(tab + s * 1024 + b1)));
                u2v y0 = __builtin_bit_cast(u2v, __builtin_amdgcn_ds_read_tr16_b64_v4i16((trp)(tab + 8192 + s * 1024 + b0)));
                u2v y1 = __builtin_bit_cast(u2v, __builtin_amdgcn_ds_read_tr16_b64_v4i16((trp)(tab + 8192 + s * 1024 + b1)));
                kd = __builtin_amdgcn_mfma_f32_32x32x16_bf16(__builtin_bit_cast(bf8, u4v{x0[0], x0[1], x1[0], x1[1]}),
                                                             __builtin_bit_cast(bf8, u4v{y0[0], y0[1], y1[0], y1[1]}), kd, 0, 0, 0);
            }
        }
        volatile LAS u32* pairf = (volatile LAS u32*)(sm + FL_OFF + 16 + 4 * (w & 1));
        if (w >= 2) {
#pragma unroll
            for (int g = 0; g < 4; ++g)
                *(LAS f4v*)(tab + g * 1024 + l * 16) = f4v{kd[4 * g], kd[4 * g + 1], kd[4 * g + 2], kd[4 * g + 3]};
            asm volatile("" ::: "memory");
            *pairf = 1u;
        } else {
            u32 spins = 0;
            while (*pairf == 0u && ++spins < SPIN_CAP) __builtin_amdgcn_s_sleep(1);
            asm volatile("" ::: "memory");
#pragma unroll
            for (int g = 0; g < 4; ++g) {
                f4v o = *(LAS f4v*)(tab + 2 * TAB_WAVE + g * 1024 + l * 16);
                kd[4 * g] += o[0]; kd[4 * g + 1] += o[1]; kd[4 * g + 2] += o[2]; kd[4 * g + 3] += o[3];
            }
            LAS char* zb = sm + Z_OFF + w * Z_CONV + Z_PAD + 16 * (l & 31);
            if (l < 32) {
                *(LAS u4v*)(zb) = u4v{pk2(kd[0], kd[1]), pk2(kd[2], kd[3]), pk2(kd[4], kd[5]), pk2(kd[6], kd[7])};
                *(LAS u4v*)(zb + 512) = u4v{pk2(kd[8], kd[8]), pk2(kd[9], kd[10]), pk2(kd[10], kd[10]), pk2(kd[11], kd[12])};
                *(LAS u4v*)(zb + 1024) = u4v{pk2(kd[13], kd[13]), pk2(kd[13], kd[14]), pk2(kd[14], kd[14]), pk2(kd[15], kd[15])};
            } else {
                *(LAS u4v*)(zb + 1536) = u4v{pk2(kd[0], kd[0]), pk2(kd[0], kd[1]), pk2(kd[1], kd[1]), pk2(kd[2], kd[2])};
#pragma unroll
                for (int a = 0; a < 4; ++a) {
                    const float lo = kd[3 + 2 * a], hi = kd[4 + 2 * a];
                    const u32 ll = pk2(lo, lo);
                    *(LAS u4v*)(zb + 2048 + 512 * a) = u4v{ll, ll, pk2((l & 1) ? hi : lo, hi), pk2(hi, hi)};
                }
            }
        }
        }
    } else {
        const int cw = w - 4;
        if (MODE != 2) {
            float s0 = 0.f, s1 = 0.f;
#pragma unroll
            for (int n = 0; n < 4; ++n) { s0 = dot2(grow0[n], gwv[n], s0); s1 = dot2(grow1[n], gwv[n], s1); }
#pragma unroll
            for (int o = 32; o > 0; o >>= 1) { s0 += __shfl_xor(s0, o); s1 += __shfl_xor(s1, o); }
            const float g0 = 1.f / (1.f + expf(-rbf(s0))), g1 = 1.f / (1.f + expf(-rbf(s1)));
            if (l == 0) __hip_atomic_store((gu64*)(ws + WS_G) + 4 * blockIdx.x + cw, (tag << 32) | pk2(g0, g1), RLX_AGENT);
        }
        if (MODE != 1) {
            if (MODE == 0) {
                gu64* fp = (gu64*)(ws + WS_FLAG) + (bx * 4 + (bj >> 3)) * 64 + l;
                u32 spins = 0;
                while (!__all(__hip_atomic_load(fp, RLX_AGENT) == tag) && ++spins < SPIN_CAP) __builtin_amdgcn_s_sleep(1);
                asm volatile("" ::: "memory");
            }
            STAMP(3);
            const u32 xo = (u32)(d0 + (cw & 1)) * 8192u + (u32)(cw >> 1) * 4096u + 16u * l;
            u4v xv[4];
#pragma unroll
            for (int pc = 3; pc >= 0; --pc) xv[pc] = __builtin_amdgcn_raw_buffer_load_b128(xrs, xo + 1024u * pc, 0, MODE == 0 ? 16 : 0);
            LAS char* xb = sm + XC_OFF + cw * XC_CONV + 16 * l;
#pragma unroll
            for (int pc = 3; pc >= 0; --pc) {
                const u32 nxt = (pc == 3) ? 0u : (u32)__builtin_amdgcn_readfirstlane(xv[pc == 3 ? 3 : pc + 1][0]);
                const u32 d4 = (u32)__builtin_amdgcn_update_dpp((int)nxt, (int)xv[pc][0], 0x130, 0xf, 0xf, false);
                *(LAS u4v*)(xb + 1024 * pc) = xv[pc];
                *(LAS u4v*)(xb + XC_COPY + 1024 * pc) =
                    u4v{__builtin_amdgcn_alignbit(xv[pc][1], xv[pc][0], 16), __builtin_amdgcn_alignbit(xv[pc][2], xv[pc][1], 16),
                        __builtin_amdgcn_alignbit(xv[pc][3], xv[pc][2], 16), __builtin_amdgcn_alignbit(d4, xv[pc][3], 16)};
            }
        }
    }
    if (MODE == 1) return;
    STAMP(4);
    asm volatile("s_waitcnt lgkmcnt(0)" ::: "memory");
    __builtin_amdgcn_s_barrier();
    STAMP(5);

    if (w < 4) {
        const LAS u32* ab = (const LAS u32*)(sm + XC_OFF + w * XC_CONV + ((l & 1) ? 0 : XC_COPY)) + (1023 - (l >> 1));
        const LAS char* bb = sm + Z_OFF + w * Z_CONV + Z_PAD + 16 * ((l & 31) - 4 * (l >> 5));
        LAS char* yb = sm + Y_OFF + w * 8192 + 16 * l;
        const u32 ycb = (u32)(size_t)(sm + YC_OFF + w * 2048 + 64 * (l >> 4));
        volatile LAS u32* done = (volatile LAS u32*)(sm + FL_OFF + 4 * w);
        constexpr int PD = 4, QRES = 8;
        u4v A[32], Br[12], Bs[8];
        f16v acc[2];
        auto ldA = [&](int i) { const LAS u32* p = ab - 32 * i; return u4v{p[0], p[1], p[2], p[3]}; };
        auto ldB = [&](int q) { return *(const LAS u4v*)(bb + 128 * q); };
        auto fetch = [&](int be, int i) {
            const int q = 4 * be - i;
            if (i >= 4 * be) A[i] = ldA(i);
            if (q > QRES) Bs[i & 7] = ldB(q);
            else if (i < 4) Br[q + 3] = ldB(q);
        };
        auto post = [&](int be, int ph) {
            f16v& a = acc[be & 1];
            if (ph < 3) {
#pragma unroll
                for (int e = 0; e < 4; ++e) a[4 * (2 - ph) + e] += dppz<0x111>(a[4 * (3 - ph) + e]);
            } else {
                *(LAS f4v*)(yb + 1024 * be) = f4v{a[0], a[1], a[2], a[3]};
                asm volatile("s_mov_b64 exec, %4\n\t"
                             "ds_write_b128 %0, %1 offset:%5\n\t"
                             "ds_write_b128 %0, %2 offset:%5+16\n\t"
                             "ds_write_b128 %0, %3 offset:%5+32\n\t"
                             "s_mov_b64 exec, -1"
                             :: "v"(ycb), "v"(f4v{a[4], a[5], a[6], a[7]}), "v"(f4v{a[8], a[9], a[10], a[11]}),
                                "v"(f4v{a[12], a[13], a[14], a[15]}), "s"(0x8000800080008000ull), "i"(256 * be) : "memory");
                *done = (u32)(be + 1);
            }
        };
#pragma unroll
        for (int i = 0; i < PD; ++i) fetch(0, i);
#pragma unroll
        for (int be = 0; be < 8; ++be) {
#pragma unroll
            for (int i = 0; i < 4 * be + 4; ++i) {
                const int q = 4 * be - i;
                const u4v bfr = (q > QRES) ? Bs[i & 7] : Br[q + 3];
                if (i == 0) acc[be & 1] = __builtin_amdgcn_mfma_f32_32x32x16_bf16(__builtin_bit_cast(bf8, A[i]), __builtin_bit_cast(bf8, bfr), f16v{}, 0, 0, 0);
                else acc[be & 1] = __builtin_amdgcn_mfma_f32_32x32x16_bf16(__builtin_bit_cast(bf8, A[i]), __builtin_bit_cast(bf8, bfr), acc[be & 1], 0, 0, 0);
                if (i + PD < 4 * be + 4) fetch(be, i + PD);
                else if (be < 7) fetch(be + 1, i + PD - 4 * be - 4);
                if (be > 0 && i >= 1 && i <= 4) post(be - 1, i - 1);
                __builtin_amdgcn_sched_barrier(0);
            }
        }
#pragma unroll
        for (int ph = 0; ph < 4; ++ph) post(7, ph);
        STAMP(6);
    } else {
        const int cw = w - 4;
        const int tl = 8 * (16 * (cw & 1) + (l >> 2)) + 4 * (cw >> 1) + (l & 3);
        float G[8], C1[8][2], C0[8][2];
        {
            gu64* gp = (gu64*)(ws + WS_G) + (tl >> 1);
            u32 gv2[8]; u32 spins = 0;
            for (;;) {
                bool ok = true;
#pragma unroll
                for (int be = 0; be < 8; ++be) {
                    const u64 xg = __hip_atomic_load(gp + 128 * be, RLX_AGENT);
                    gv2[be] = (u32)xg; ok &= ((u32)(xg >> 32) == (u32)tag);
                }
                if (__all(ok) || ++spins >= SPIN_CAP) break;
                __builtin_amdgcn_s_sleep(1);
            }
            if (MODE == 0 && blockIdx.x == 0) {
                if (l == 0) {
                    const u32 old = __hip_atomic_fetch_add((LAS u32*)(sm + FL_OFF + 32), 1u, __ATOMIC_RELAXED, __HIP_MEMORY_SCOPE_WORKGROUP);
                    if (old == 3u) __hip_atomic_store((gu64*)ws, tag, RLX_AGENT);
                }
            }
            const float ds0 = bf2f(gdsk[d0]), ds1 = bf2f(gdsk[d0 + 1]), sd0 = bf2f(gssd[d0]), sd1 = bf2f(gssd[d0 + 1]);
            const LAS u16* xr = (const LAS u16*)(sm + XC_OFF) + (2047 - tl);
#pragma unroll
            for (int be = 0; be < 8; ++be) {
                const float g = (tl & 1) ? __uint_as_float(gv2[be] & 0xffff0000u) : bf2f(gv2[be] & 0xffffu);
                const float og = rbf(1.f - g);
                G[be] = g;
#pragma unroll
                for (int c = 0; c < 2; ++c) {
                    const float uu = bf2f(xr[c * (XC_CONV / 2) - 256 * be]);
                    const float kv = bf2f(xr[(2 + c) * (XC_CONV / 2) - 256 * be]);
                    const float c1 = og * uu;
                    C1[be][c] = c1;
                    C0[be][c] = g * uu * (c ? ds1 : ds0) + c1 * kv * (c ? sd1 : sd0);
                }
            }
        }
        u32* op = (u32*)(gout + (size_t)tl * DM + d0);
        const LAS float* ym = (const LAS float*)(sm + Y_OFF) + 64 * cw + l;
        const LAS float* yc = (const LAS float*)(sm + YC_OFF) + ((cw & 1) ? 16 * (cw - 1) : 16 * (cw + 1) - 64) + (l < 12 ? l : 0);
        volatile LAS u4v* dn = (volatile LAS u4v*)(sm + FL_OFF);
#pragma unroll
        for (int be = 0; be < 8; ++be) {
            u32 spins = 0;
            for (;;) {
                const u4v d = *(const LAS u4v*)dn;
                asm volatile("" ::: "memory");
                const u32 m = min(min(d[0], d[1]), min(d[2], d[3]));
                if (__builtin_amdgcn_readfirstlane(m) > (u32)be || ++spins >= SPIN_CAP) break;
                __builtin_amdgcn_s_sleep(1);
            }
            float y[4];
#pragma unroll
            for (int cv = 0; cv < 4; ++cv) {
                y[cv] = ym[cv * 2048 + 256 * be];
                float cr = yc[cv * 512 + 64 * be];
                if (l >= 12 || (be == 0 && !(cw & 1))) cr = 0.f;
                y[cv] += cr;
            }
            const float o0 = G[be] * y[0] + (C1[be][0] * y[2] + C0[be][0]);
            const float o1 = G[be] * y[1] + (C1[be][1] * y[3] + C0[be][1]);
            op[(size_t)be * 256 * (DM / 2)] = pk2(o0, o1);
        }
        STAMP(6);
    }
}

#define KMODE 0
extern "C" void kernel_launch(void* const* d_in, const int* in_sizes, int n_in,
                              void* d_out, int out_size, void* d_ws, size_t ws_size,
                              hipStream_t stream) {
#define ARGS (const u16*)d_in[0], (const u16*)d_in[1], (const u16*)d_in[2], (const u16*)d_in[3], (const u16*)d_in[4], \
             (const u16*)d_in[5], (const u16*)d_in[6], (const u16*)d_in[8], (const u16*)d_in[9], (const u16*)d_in[10], \
             (const u16*)d_in[11], (const u16*)d_in[12], (const u16*)d_in[13], (u16*)d_out, (char*)d_ws
    fused_k<0><<<256, 512, 0, stream>>>(ARGS);
}

#undef kernel_launch
typedef __attribute__((address_space(4))) const unsigned long long* kap;
template<int BIG>
__global__ void __launch_bounds__(BIG ? 512 : 64) x_k(char* ws_unused) {
    __shared__ unsigned s[BIG ? 39000 : 64];
    unsigned long long t0, t1;
    kap ka = (kap)__builtin_amdgcn_kernarg_segment_ptr();
    asm volatile("s_memrealtime %0\n\ts_waitcnt lgkmcnt(0)" : "=s"(t0), "+s"(ka) :: "memory");
    char* ws = (char*)ka[0];
    asm volatile("s_memrealtime %0\n\ts_waitcnt lgkmcnt(0)" : "=s"(t1) : "s"(ws) : "memory");
    if (BIG) { asm volatile("v_mov_b32 v213, 0" ::: "v213"); s[threadIdx.x] = 1; if (s[threadIdx.x ^ 1] == 12345) t1 = 0; }
    unsigned long long t2, t3;
    unsigned long long v = *(volatile unsigned long long*)(ws + (20u << 20) + blockIdx.x * 4096);
    asm volatile("s_waitcnt vmcnt(0)\n\ts_memrealtime %0\n\ts_waitcnt lgkmcnt(0)" : "=s"(t2) : "v"(v) : "memory");
    if (threadIdx.x == 0) { unsigned long long* p = (unsigned long long*)(ws + (16u << 20)) + blockIdx.x * 4; p[0] = t0; p[1] = t1; p[2] = t2; }
}
__global__ void __launch_bounds__(64) diag_k(char* ws) {
    const unsigned long long* hs = (const unsigned long long*)(ws + (16u << 20));
    if (threadIdx.x == 0) {
        unsigned long long mn = ~0ull, mx = 0, s1 = 0, s2 = 0;
        for (int i = 0; i < NB; ++i) { mn = min(mn, hs[i * 4]); mx = max(mx, hs[i * 4]); s1 += hs[i * 4 + 1] - hs[i * 4]; s2 += hs[i * 4 + 2] - hs[i * 4 + 1]; }
        unsigned long long v = WHAT == 0 ? 0 : WHAT == 1 ? s1 / NB : WHAT == 2 ? s2 / NB : mx - mn;
        if (v > 4000) v = 4000;
        unsigned long long until = mn + 20000 + v * 20;
        while (__builtin_amdgcn_s_memrealtime() < until) __builtin_amdgcn_s_sleep(1);
    }
}
extern "C" void kernel_launch(void* const* d_in, const int* in_sizes, int n_in, void* d_out, int out_size, void* d_ws, size_t ws_size, hipStream_t stream) {
    x_k<BIGX><<<NB, BIGX ? 512 : 64, 0, stream>>>((char*)d_ws);
    kernel_launch_inner(d_in, in_sizes, n_in, d_out, out_size, d_ws, ws_size, stream);
    diag_k<<<1, 64, 0, stream>>>((char*)d_ws);
}
```

```cpp
#include <hip/hip_runtime.h>
#include <stdint.h>

typedef unsigned short u16;
typedef unsigned int u32;
typedef unsigned long long u64;
typedef __attribute__((ext_vector_type(2))) __bf16 bf2;
typedef __attribute__((ext_vector_type(8))) __bf16 bf8;
typedef __attribute__((ext_vector_type(4))) short s4v;
typedef __attribute__((ext_vector_type(2))) u32 u2v;
typedef __attribute__((ext_vector_type(3))) u32 u3v;
typedef __attribute__((ext_vector_type(4))) u32 u4v;
typedef __attribute__((ext_vector_type(2))) float f2v;
typedef __attribute__((ext_vector_type(4))) float f4v;
typedef __attribute__((ext_vector_type(16))) float f16v;
#define GAS __attribute__((address_space(1)))
#define LAS __attribute__((address_space(3)))
typedef GAS u64 gu64;
#define RLX_AGENT __ATOMIC_RELAXED, __HIP_MEMORY_SCOPE_AGENT

#define STAMP_DECL
#define STAMP(i)
#define STAMP_DUMP
#define NT " nt"
#define POLL_SLEEP0 12
#define POLL_SLEEP 2
#define CODE_PF 2
#define BSPLIT 1
#define HPRIO 0
#define GSCOPE __HIP_MEMORY_SCOPE_AGENT
#define CODE_BYTES 15104
#define QRES 14
#define RS0 3
#define GEARLY 1
#define TOUCH 1

constexpr int SEQ = 2048, DM = 512, NS = 128;

constexpr int XC_OFF = 0, XC_CONV = 8384, XC_COPY = 4160;
constexpr int Z_OFF = 33536, Z_CONV = 4608, Z_PAD = 512;
constexpr int Y_OFF = 51968, Y_CONV = 8256;
constexpr int TAB_OFF = 84992, TAB_CH = 32768;
constexpr int FL_OFF = 150528;
constexpr int LDS_BYTES = 150912;
constexpr int TU_OFF = Y_OFF, TV_OFF = Y_OFF + 8192, TK_OFF = Y_OFF + 16384;
constexpr int WS_FLAG = 16384, WS_G = 65536, WS_X = 1 << 20;
constexpr u32 SPIN_CAP = 1u << 20;

__device__ __forceinline__ float bf2f(u32 x) { return __uint_as_float(x << 16); }
__device__ __forceinline__ float bfhi(u32 x) { return __uint_as_float(x & 0xffff0000u); }
__device__ __forceinline__ u32 pk2(float a, float b) { bf2 r; r[0] = (__bf16)a; r[1] = (__bf16)b; return __builtin_bit_cast(u32, r); }
__device__ __forceinline__ float rbf(float a) { return __uint_as_float(pk2(0.f, a)); }
__device__ __forceinline__ float dot2(u32 a, u32 b, float c) {
    return __builtin_amdgcn_fdot2_f32_bf16(__builtin_bit_cast(bf2, a), __builtin_bit_cast(bf2, b), c, false);
}
struct cmult { f2v b, bs; };
__device__ __forceinline__ cmult mkm(f2v b) { return cmult{b, f2v{-b[1], b[0]}}; }
__device__ __forceinline__ f2v cmul(f2v a, const cmult& m) {
    return __builtin_elementwise_fma(f2v{a[0], a[0]}, m.b, f2v{a[1], a[1]} * m.bs);
}
__device__ __forceinline__ f2v cexp_p(float lz, float rev, float p) {
    const float m = __builtin_amdgcn_exp2f(lz * p);
    const float a = __builtin_amdgcn_fractf(rev * p);
    return f2v{m * __builtin_amdgcn_cosf(a), m * __builtin_amdgcn_sinf(a)};
}
template <int CTRL> __device__ __forceinline__ float dppz(float v) {
    return __int_as_float(__builtin_amdgcn_update_dpp(0, __float_as_int(v), CTRL, 0xf, 0xf, true));
}
template <int I> struct ic { static constexpr int v = I; };
template <int B, int E, class F> __device__ __forceinline__ void sfor(F&& f) {
    if constexpr (B < E) { f(ic<B>{}); sfor<B + 1, E>(f); }
}
__device__ __forceinline__ void dma16(const void* base, u32 off, u32 lds) {
    u32 keep;
    asm volatile("s_mov_b32 %0, m0\n\ts_mov_b32 m0, %3\n\ts_nop 0\n\tglobal_load_lds_dwordx4 %1, %2" NT "\n\ts_mov_b32 m0, %0"
                 : "=&s"(keep) : "v"(off), "s"(base), "s"(lds) : "memory");
}

__global__ void __launch_bounds__(512, 2)
fused_k(const u16* __restrict__ gu, const u16* __restrict__ glar, const u16* __restrict__ gaim,
        const u16* __restrict__ gcre, const u16* __restrict__ gcim, const u16* __restrict__ gldt,
        const u16* __restrict__ gdsk, const u16* __restrict__ gk, const u16* __restrict__ gv,
        const u16* __restrict__ gshk, const u16* __restrict__ gssk, const u16* __restrict__ gssd,
        const u16* __restrict__ ggw, u16* __restrict__ gout, char* ws)
{
    __shared__ __attribute__((aligned(16))) char smem[LDS_BYTES];
    LAS char* sm = (LAS char*)smem;
    const u32 smb = (u32)(size_t)sm;
    const int tid = threadIdx.x, l = tid & 63;
    const int w = __builtin_amdgcn_readfirstlane(tid >> 6);
    const int bx = blockIdx.x & 7, bj = blockIdx.x >> 3;
    const int d0 = 64 * bx + 2 * bj;
    STAMP_DECL;
    STAMP(0);
    u32 pfd;
    {
        u64 pc;
        asm volatile("s_getpc_b64 %0" : "=s"(pc));
        asm volatile("global_load_dword %0, %1, %2" : "=v"(pfd) : "v"(min((u32)(tid & (CODE_PF * 64 - 1)) * 128u, (u32)CODE_BYTES)), "s"(pc) : "memory");
    }
    asm volatile("" :: "s"(gdsk), "s"(gssk), "s"(gssd), "s"(ggw), "s"(gout), "s"(ws));
    const __amdgpu_buffer_rsrc_t xrs = __builtin_amdgcn_make_buffer_rsrc(ws + WS_X, 0, DM * 2 * 4096, 0x00020000);

    u32 p_lar, p_aim, p_cre, p_cim, p_ldt, skip; u2v shk; u64 tagv;
    {
        const int r = 8 * w + (l >> 3);
        const int f = ((r >> 1) & 1) | (((r >> 3) & 1) << 1);
        const u32 go = (u32)(64 * bj + r) * (DM * 2) + 128 * bx + 16 * ((l & 7) ^ (f << 1));
        if (w == 1 && bj > 0) dma16(gk, go - 16 * DM * 2, smb + TK_OFF);
        const u32 o = 2u * (u32)((d0 + (w & 1)) * NS + 64 * ((w >> 1) & 1) + l);
        const u32 od = 2u * (u32)(d0 + (w & 1));
        asm volatile("global_load_ushort %0, %5, %7" NT "\n\tglobal_load_ushort %1, %6, %8" NT "\n\tglobal_load_ushort %2, %5, %9" NT "\n\t"
                     "global_load_ushort %3, %5, %10" NT "\n\tglobal_load_ushort %4, %5, %11" NT
                     : "=&v"(p_lar), "=&v"(p_ldt), "=&v"(p_aim), "=&v"(p_cre), "=&v"(p_cim)
                     : "v"(o), "v"(od), "s"(glar), "s"(gldt), "s"(gaim), "s"(gcre), "s"(gcim) : "memory");
        dma16(gk, go, smb + TK_OFF + 1024 + 1024 * w);
        dma16(gv, go, smb + TV_OFF + 1024 * w);
        dma16(gu, go, smb + TU_OFF + 1024 * w);
        const u32 os = 8u * (u32)(64 * bx + 16 * (w & 3) + (l & 15));
        asm volatile("global_load_dwordx2 %0, %3, %6" NT "\n\tglobal_load_dwordx2 %1, %4, %7 sc1\n\tglobal_load_ushort %2, %5, %8"
                     : "=&v"(shk), "=&v"(tagv), "=&v"(skip) : "v"(os), "v"(0u), "v"(od), "s"(gshk), "s"(ws), "s"((w & 2) ? gssd : gdsk) : "memory");
        const u4v z4{0, 0, 0, 0};
        const int zo = (w < 4) ? ((l < 32) ? Z_OFF + w * Z_CONV + 16 * l
                                           : (l < 35) ? XC_OFF + w * XC_CONV + 4096 + (l == 32 ? 0 : XC_COPY - 16 * (l - 33)) : FL_OFF + 16 * w)
                               : FL_OFF + 64;
        *(LAS u4v*)(sm + ((w == 5 && bj == 0) ? TK_OFF + 16 * l : zo)) = z4;
    }
    asm volatile("s_waitcnt vmcnt(6)" : "+v"(p_lar), "+v"(p_ldt), "+v"(p_aim), "+v"(p_cre), "+v"(p_cim) :: "memory");
    STAMP(1);
    u4v gr0, gr1, gwv;
    if (w < 4) {
        const u32 ro = (u32)(8 * blockIdx.x + 2 * w) * (DM * 2) + 16 * l;
        asm volatile("global_load_dwordx4 %0, %3, %5" NT "\n\tglobal_load_dwordx4 %1, %3, %5 offset:1024" NT "\n\tglobal_load_dwordx4 %2, %4, %6"
                     : "=&v"(gr0), "=&v"(gr1), "=&v"(gwv) : "v"(ro), "v"(16u * l), "s"(gu), "s"(ggw) : "memory");
    }
    LAS char* tab = sm + TAB_OFF + (w & 1) * TAB_CH + ((w >> 1) & 1) * 8192 + l * 64;
    const int sw = (l >> 1) & 3;
    cmult E64; f2v e[8];
    auto btab = [&](int cc) {
        if (cc > 0) {
#pragma unroll
            for (int g = 0; g < 8; ++g) e[g] = cmul(e[g], E64);
        }
        *(LAS u4v*)(tab + 16384 + ((cc ^ sw) << 4)) = u4v{pk2(e[0][0], e[1][0]), pk2(e[2][0], e[3][0]), pk2(e[4][0], e[5][0]), pk2(e[6][0], e[7][0])};
        *(LAS u4v*)(tab + 16384 + 4096 + ((cc ^ sw) << 4)) = u4v{pk2(e[0][1], e[1][1]), pk2(e[2][1], e[3][1]), pk2(e[4][1], e[5][1]), pk2(e[6][1], e[7][1])};
    };
    {
        const float dt = rbf(expf(bf2f(p_ldt)));
        const float zr = -rbf(expf(bf2f(p_lar))) * dt;
        const float zi = bf2f(p_aim) * dt;
        const float lz = zr * 1.44269504088896341f, rev = zi * 0.15915494309189535f;
        const f2v e8 = cexp_p(lz, rev, 8.f);
        const cmult E8 = mkm(e8);
        if (w >= 4) {
            E64 = mkm(cexp_p(lz, rev, 64.f));
            e[0] = f2v{1.f, 0.f}; e[1] = e8;
            e[4] = cexp_p(lz, rev, 32.f);
            e[2] = cmul(e[1], E8); e[5] = cmul(e[4], E8); e[3] = cmul(e[2], E8);
            e[6] = cmul(e[5], E8); e[7] = cmul(e[6], E8);
#pragma unroll
            for (int cc = 0; cc < BSPLIT; ++cc) btab(cc);
        } else {
            const cmult E1 = mkm(cexp_p(lz, rev, 1.f)), E4 = mkm(cexp_p(lz, rev, 4.f)), E256 = mkm(cexp_p(lz, rev, 256.f));
            f2v a0[16], a1[16];
            a0[0] = f2v{2.f * (bf2f(p_cre) * dt), 2.f * (bf2f(p_cim) * dt)};
            a0[4] = cmul(a0[0], E4);
#pragma unroll
            for (int g = 1; g < 4; ++g) { a0[g] = cmul(a0[g - 1], E1); a0[g + 4] = cmul(a0[g + 3], E1); }
            a0[8] = cmul(a0[0], E256); a0[9] = cmul(a0[2], E256); a0[10] = cmul(a0[4], E256); a0[11] = cmul(a0[6], E256);
            a0[12] = cmul(a0[8], E8);
            a0[13] = cmul(a0[8], E256); a0[14] = cmul(a0[10], E256); a0[15] = cmul(a0[12], E256);
            a1[0] = cmul(a0[13], E256); a1[1] = cmul(a0[14], E256); a1[2] = cmul(a0[15], E256);
            a1[3] = cmul(a1[0], E256); a1[4] = cmul(a1[2], E256);
            a1[5] = cmul(a1[3], E256); a1[6] = cmul(a1[4], E256);
            a1[7] = cmul(a1[5], E256); a1[8] = cmul(a1[6], E256);
            a1[9] = cmul(a1[7], E256); a1[10] = cmul(a1[8], E256);
#pragma unroll
            for (int g = 11; g < 16; ++g) a1[g] = f2v{0.f, 0.f};
#pragma unroll
            for (int cc = 0; cc < 4; ++cc) {
                const int g = 4 * cc;
                *(LAS u4v*)(tab + ((cc ^ sw) << 4)) = u4v{pk2(a0[g][0], a0[g + 1][0]), pk2(a0[g + 2][0], a0[g + 3][0]), pk2(a1[g][0], a1[g + 1][0]), pk2(a1[g + 2][0], a1[g + 3][0])};
                *(LAS u4v*)(tab + 4096 + ((cc ^ sw) << 4)) = u4v{pk2(-a0[g][1], -a0[g + 1][1]), pk2(-a0[g + 2][1], -a0[g + 3][1]), pk2(-a1[g][1], -a1[g + 1][1]), pk2(-a1[g + 2][1], -a1[g + 3][1])};
            }
        }
    }
    STAMP(2);
    if (w >= 4) asm volatile("s_waitcnt vmcnt(0)" ::: "memory");
    else asm volatile("s_waitcnt vmcnt(3)" ::: "memory");
    asm volatile("s_waitcnt lgkmcnt(0)" : "+v"(shk), "+v"(tagv), "+v"(skip), "+v"(pfd) :: "memory");
    __builtin_amdgcn_s_barrier();
    STAMP(3);
    const u64 tag = (((u64)(u32)__builtin_amdgcn_readfirstlane((u32)(tagv >> 32)) << 32) | (u32)__builtin_amdgcn_readfirstlane((u32)tagv)) + 1ull;

    {
        const int cg = w & 3, th = w >> 2, G = l >> 4, q = (l >> 2) & 3, p = l & 3;
        const int row = 32 * th + 8 * G + 7 - q;
        const int f = ((row >> 1) & 1) | (((row >> 3) & 1) << 1);
        const int a0 = row * 128 + ((cg ^ f) << 5) + 8 * p;
        typedef LAS s4v* trp;
        s4v u0 = __builtin_amdgcn_ds_read_tr16_b64_v4i16((trp)(sm + TU_OFF + a0));
        s4v u1 = __builtin_amdgcn_ds_read_tr16_b64_v4i16((trp)(sm + TU_OFF + a0 - 512));
        s4v k0 = __builtin_amdgcn_ds_read_tr16_b64_v4i16((trp)(sm + TK_OFF + 1024 + a0));
        s4v k1 = __builtin_amdgcn_ds_read_tr16_b64_v4i16((trp)(sm + TK_OFF + 1024 + a0 - 512));
        s4v k2 = __builtin_amdgcn_ds_read_tr16_b64_v4i16((trp)(sm + TK_OFF + (a0 ^ 64)));
        s4v v0 = __builtin_amdgcn_ds_read_tr16_b64_v4i16((trp)(sm + TV_OFF + a0));
        s4v v1 = __builtin_amdgcn_ds_read_tr16_b64_v4i16((trp)(sm + TV_OFF + a0 - 512));
        const u32 xo = (u32)(bx * 32 + bj) * 16384u + (u32)((8 * cg + ((l & 15) >> 1)) * 512 + th * 256 + (l & 1) * 64 + G * 16);
        u2v ua = __builtin_bit_cast(u2v, u0), ub = __builtin_bit_cast(u2v, u1);
        __builtin_amdgcn_raw_buffer_store_b128(u4v{ua[0], ua[1], ub[0], ub[1]}, xrs, xo, 0, 16);
        u2v ka = __builtin_bit_cast(u2v, k0), kb = __builtin_bit_cast(u2v, k1), kc = __builtin_bit_cast(u2v, k2);
        u2v va = __builtin_bit_cast(u2v, v0), vb = __builtin_bit_cast(u2v, v1);
        u32 K[6] = {ka[0], ka[1], kb[0], kb[1], kc[0], kc[1]};
        u32 V[4] = {va[0], va[1], vb[0], vb[1]};
        u32 Ks[5];
#pragma unroll
        for (int n = 0; n < 5; ++n) Ks[n] = __builtin_amdgcn_alignbit(K[n + 1], K[n], 16);
        u4v kv;
#pragma unroll
        for (int n = 0; n < 4; ++n) {
            float e0 = dot2(K[n], shk[0], dot2(K[n + 1], shk[1], 0.f));
            float e1 = dot2(Ks[n], shk[0], dot2(Ks[n + 1], shk[1], 0.f));
            kv[n] = pk2(e0 * bf2f(V[n]), e1 * bfhi(V[n]));
        }
        __builtin_amdgcn_raw_buffer_store_b128(kv, xrs, xo + 128u, 0, 16);
        STAMP(4);
        if (w >= 4) {
#pragma unroll
            for (int cc = BSPLIT; cc < 4; ++cc) btab(cc);
            asm volatile("s_waitcnt vmcnt(0)" ::: "memory");
        }
        else {
            asm volatile("s_waitcnt vmcnt(2)" : "+v"(gr0), "+v"(gr1), "+v"(gwv) :: "memory");
            const float s0 = dot2(gr0[0], gwv[0], dot2(gr0[1], gwv[1], dot2(gr0[2], gwv[2], dot2(gr0[3], gwv[3], 0.f))));
            const float s1 = dot2(gr1[0], gwv[0], dot2(gr1[1], gwv[1], dot2(gr1[2], gwv[2], dot2(gr1[3], gwv[3], 0.f))));
            float ee = ((l & 1) ? s1 : s0) + dppz<0xB1>((l & 1) ? s0 : s1);
            ee += dppz<0x4E>(ee); ee += dppz<0x124>(ee); ee += dppz<0x128>(ee);
            ee += __shfl_xor(ee, 16); ee += __shfl_xor(ee, 32);
            const float g = __builtin_amdgcn_rcpf(1.f + __builtin_amdgcn_exp2f(-1.44269504088896341f * rbf(ee)));
            const float gn = dppz<0xB1>(g);
            const int row = 8 * blockIdx.x + 2 * w;
            const int be = row >> 8, cc = (row >> 3) & 31, hh = (row >> 2) & 1;
            const int gi = (((cc >> 4) + 2 * hh) * 8 + be) * 32 + (cc & 15) * 2 + (w & 1);
            if (l == 0) __hip_atomic_store((gu64*)(ws + WS_G) + gi, (tag << 32) | pk2(g, gn), RLX_AGENT);
            asm volatile("s_waitcnt vmcnt(1)" ::: "memory");
        }
        if (l == 0) __hip_atomic_store((gu64*)(ws + WS_FLAG + ((bx * 4 + cg) * 4 + (bj >> 3)) * 128) + (bj & 7) * 2 + th, tag, RLX_AGENT);
    }
    STAMP(5);
    if (w == 2 || w == 3) {
        const u32 so = (u32)(d0 + (w & 1)) * (SEQ * 2) + 16 * l;
        const u32 zd = smb + Z_OFF + w * Z_CONV + Z_PAD;
#pragma unroll
        for (int pc = 0; pc < 4; ++pc) dma16(gssk, so + 1024 * pc, zd + 1024 * pc);
    }
    asm volatile("s_waitcnt lgkmcnt(0)" ::: "memory");
    __builtin_amdgcn_s_barrier();

    if (w < 4) {
        if (w < 2) {
            LAS char* tab = sm + TAB_OFF + w * TAB_CH;
            f16v kd = {};
            const int h = l >> 5, blk = (l >> 4) & 1, q = (l >> 2) & 3, p = l & 3;
            typedef LAS s4v* trp;
            const int b0 = (8 * h + q) * 64 + (((2 * blk + (p >> 1)) ^ (q >> 1)) << 4) + 8 * (p & 1);
            const int b1 = (8 * h + 4 + q) * 64 + (((2 * blk + (p >> 1)) ^ (2 + (q >> 1))) << 4) + 8 * (p & 1);
#pragma unroll
            for (int s = 0; s < 16; ++s) {
                u2v x0 = __builtin_bit_cast(u2v, __builtin_amdgcn_ds_read_tr16_b64_v4i16((trp)(tab + s * 1024 + b0)));
                u2v x1 = __builtin_bit_cast(u2v, __builtin_amdgcn_ds_read_tr16_b64_v4i16((trp)(tab + s * 1024 + b1)));
                u2v y0 = __builtin_bit_cast(u2v, __builtin_amdgcn_ds_read_tr16_b64_v4i16((trp)(tab + 16384 + s * 1024 + b0)));
                u2v y1 = __builtin_bit_cast(u2v, __builtin_amdgcn_ds_read_tr16_b64_v4i16((trp)(tab + 16384 + s * 1024 + b1)));
                kd = __builtin_amdgcn_mfma_f32_32x32x16_bf16(__builtin_bit_cast(bf8, u4v{x0[0], x0[1], x1[0], x1[1]}),
                                                             __builtin_bit_cast(bf8, u4v{y0[0], y0[1], y1[0], y1[1]}), kd, 0, 0, 0);
            }
            if (l == 0) kd[0] += bf2f(skip);
            LAS char* zb = sm + Z_OFF + w * Z_CONV + Z_PAD + 16 * (l & 31);
            if (l < 32) {
                *(LAS u4v*)(zb) = u4v{pk2(kd[0], kd[1]), pk2(kd[2], kd[3]), pk2(kd[4], kd[5]), pk2(kd[6], kd[7])};
                *(LAS u4v*)(zb + 512) = u4v{pk2(kd[8], kd[8]), pk2(kd[9], kd[10]), pk2(kd[10], kd[10]), pk2(kd[11], kd[12])};
                *(LAS u4v*)(zb + 1024) = u4v{pk2(kd[13], kd[13]), pk2(kd[13], kd[14]), pk2(kd[14], kd[14]), pk2(kd[15], kd[15])};
            } else {
                *(LAS u4v*)(zb + 1536) = u4v{pk2(kd[0], kd[0]), pk2(kd[0], kd[1]), pk2(kd[1], kd[1]), pk2(kd[2], kd[2])};
#pragma unroll
                for (int a = 0; a < 4; ++a) {
                    const float lo = kd[3 + 2 * a], hi = kd[4 + 2 * a];
                    const u32 ll = pk2(lo, lo);
                    *(LAS u4v*)(zb + 2048 + 512 * a) = u4v{ll, ll, pk2((l & 1) ? hi : lo, hi), pk2(hi, hi)};
                }
            }
        }
        if (w >= 2) {
            asm volatile("s_waitcnt vmcnt(0)" ::: "memory");
            LAS u16* z0 = (LAS u16*)(sm + Z_OFF + w * Z_CONV + Z_PAD);
            if (l == 0) *z0 = (u16)pk2(bf2f(*z0) + bf2f(skip), 0.f);
        }
        STAMP(6);

        u32 abase[4];
#pragma unroll
        for (int g = 0; g < 4; ++g) {
            abase[g] = smb + XC_OFF + w * XC_CONV + ((l & 1) ? 0 : XC_COPY) + 4 * (1023 - (l >> 1) - 256 * g - 224);
            asm volatile("" : "+v"(abase[g]));
        }
        const LAS char* bb = sm + Z_OFF + w * Z_CONV + Z_PAD + 16 * ((l & 31) - 4 * (l >> 5));
        LAS char* yb = sm + Y_OFF + w * Y_CONV + 8 * l;
        u32 done_ = smb + FL_OFF + (l == 0 ? w : 128 + 4 * l); asm volatile("" : "+v"(done_));
        volatile LAS unsigned char* done = (volatile LAS unsigned char*)(size_t)done_;
        const int chb = l & 1;
        const int ta = 8 * (16 * (w & 1) + (l >> 2)) + 4 * (w >> 1) + (l & 2);
        const u32 oo = (u32)(ta + chb) * (DM * 2) + 2 * d0;
        const __amdgpu_buffer_rsrc_t ors = __builtin_amdgcn_make_buffer_rsrc(gout, 0, SEQ * DM * 2, 0x00020000);
        const LAS char* ym = sm + Y_OFF + chb * Y_CONV + 8 * (16 * w + (l >> 2)) + 2 * (l & 2);
        const LAS char* yc = sm + Y_OFF + chb * Y_CONV + 512 + ((w & 1) ? 128 * (w - 1) : 128 * (w + 1) - 1024) + 8 * (7 + (l < 12 ? (l & 12) : 0)) + 2 * (l & 2);
        const LAS char* xr = sm + XC_OFF + chb * XC_CONV + 2 * (2046 - ta) - 512 * 7;
        const float cmask = l < 12 ? 1.f : 0.f;
        const u32 sel = chb ? 0x07060302u : 0x01000504u;
        const LAS char* gl = sm + TAB_OFF + 256 * w + 4 * l;
        constexpr int PD = 5;
        u32 flp_ = smb + FL_OFF; asm volatile("" : "+v"(flp_));
        const LAS char* flp = (const LAS char*)(size_t)flp_;
        u4v A[32], Br[QRES + 4], Bs[8];
        f16v acc[2];
        u32 G2[8], fh[8];
        u32 W0, W1, T0, T1, T2, T3;
        u32 fl[8], uu[8], ma[8], mb[8], ca[8], cb[8], P; float x[8], ga, gb, c1a, c1b;
#define PIN(v) asm("" : "+v"(v))
        auto ldA = [&](int i, int h) __attribute__((always_inline)) {
            const LAS u32* p = (const LAS u32*)(size_t)abase[i >> 3] + (224 - 32 * (i & 7)) + 2 * h;
            A[i][2 * h] = p[0]; A[i][2 * h + 1] = p[1];
        };
        auto ldB = [&](int q) __attribute__((always_inline)) { return *(const LAS u4v*)(bb + 128 * q); };
        auto horner = [&](int be, int n) __attribute__((always_inline)) {
            f16v& a = acc[be & 1];
            const int e = 8 - 4 * (n >> 2) + (n & 3);
            float t = a[e] + dppz<0x111>(a[e + 4]); PIN(t); a[e] = t;
        };
        auto post = [&](int be, int ph) __attribute__((always_inline)) {
            f16v& a = acc[be & 1];
            if (ph < 2) {
#pragma unroll
                for (int n = 5 * ph; n < 5 * ph + 5; ++n) horner(be, n);
            } else if (ph == 2) {
                horner(be, 10); horner(be, 11);
                W0 = pk2(a[12], a[13]); W1 = pk2(a[14], a[15]); T0 = pk2(a[8], a[9]); T1 = pk2(a[10], a[11]);
            } else if (ph == 3) {
                W0 = (u32)__builtin_amdgcn_update_dpp((int)W0, (int)T0, 0x104, 0xf, 0x4, false);
                W1 = (u32)__builtin_amdgcn_update_dpp((int)W1, (int)T1, 0x104, 0xf, 0x4, false);
                *(LAS u2v*)(yb + 1024 * be) = u2v{pk2(a[0], a[1]), pk2(a[2], a[3])};
                T2 = pk2(a[4], a[5]); T3 = pk2(a[6], a[7]);
            } else if (ph == 4) {
                W0 = (u32)__builtin_amdgcn_update_dpp((int)W0, (int)T2, 0x108, 0xf, 0x2, false);
                W1 = (u32)__builtin_amdgcn_update_dpp((int)W1, (int)T3, 0x108, 0xf, 0x2, false);
                asm volatile("" ::: "memory");
                *(LAS u2v*)(yb + 512 + 1024 * be) = u2v{W0, W1};
            } else if (ph == 5) {
                asm volatile("" ::: "memory"); *done = (unsigned char)(be + 1);
            }
        };
        auto rd = [&](int be, int h) __attribute__((always_inline)) {
            if (h == 0) {
                if (be == 0) { const u2v f2 = *(const volatile LAS u2v*)flp; fl[be] = f2[0]; fh[be] = f2[1]; }
                else fl[be] = *(const volatile LAS u32*)flp;
                asm volatile("" ::: "memory");
                ma[be] = *(const LAS u32*)(ym + 1024 * be); mb[be] = *(const LAS u32*)(ym + 2 * Y_CONV + 1024 * be);
            } else {
                ca[be] = *(const LAS u32*)(yc + 1024 * be); cb[be] = *(const LAS u32*)(yc + 2 * Y_CONV + 1024 * be);
                if (be == 0) G2[be] = *(const LAS u32*)gl;
            }
        };
        auto rdu = [&](int be) __attribute__((always_inline)) {
            uu[be] = *(const LAS u32*)(xr + 512 * (7 - be));
            if (be > 0) G2[be] = *(const LAS u32*)(gl + 1024 * be);
        };
        u32 sfl;
        auto grab = [&](int be) __attribute__((always_inline)) {
            asm volatile("" : "+v"(ma[be]), "+v"(mb[be]), "+v"(ca[be]), "+v"(cb[be]), "+v"(fl[be]));
            if (be == 0) { asm volatile("" : "+v"(G2[0])); fl[0] &= fh[0]; }
            sfl = (u32)__builtin_amdgcn_readfirstlane(fl[be]);
        };
        auto ready = [&](int be) __attribute__((always_inline)) {
            return ((sfl + (u32)(0x7f - be) * 0x01010101u) & 0x80808080u) == 0x80808080u;
        };
        auto coef = [&](int be, int h) __attribute__((always_inline)) {
            if (h == 0) { ga = bf2f(G2[be]); gb = bfhi(G2[be]); c1a = 1.f - ga; PIN(c1a); c1b = 1.f - gb; PIN(c1b); }
            else { c1a *= bfhi(uu[be]); PIN(c1a); c1b *= bf2f(uu[be]); PIN(c1b); }
        };
        auto mix = [&](int be, int ph) __attribute__((always_inline)) {
            if (ph == -2) { grab(be); if (be > 0 && be < 7) coef(be, 0); }
            else if (ph == -1) {
                if (__builtin_expect(!ready(be), 0)) {
#pragma nounroll
                    for (u32 spins = 0; spins < SPIN_CAP; ++spins) { rd(be, 0); rd(be, 1); grab(be); if (ready(be)) break; }
                }
            } else if (ph == 0) { if (be == 0) coef(be, 0); }
            else if (ph == 1) { if (be < 7) coef(be, 1);
            } else if (ph == 2) {
                if (be == 0) { if (!(w & 1)) { ca[be] = 0u; cb[be] = 0u; } }
                x[0] = bf2f(mb[be]); x[1] = bfhi(mb[be]); x[2] = bf2f(cb[be]); x[3] = bfhi(cb[be]); x[4] = bf2f(ma[be]);
            } else if (ph == 3) {
                x[0] += cmask * x[2]; PIN(x[0]); x[1] += cmask * x[3]; PIN(x[1]);
                x[5] = bfhi(ma[be]); x[6] = bf2f(ca[be]); x[7] = bfhi(ca[be]);
            } else if (ph == 4) {
                x[4] += cmask * x[6]; PIN(x[4]); x[5] += cmask * x[7]; PIN(x[5]);
                x[0] *= c1a; PIN(x[0]); x[1] *= c1b; PIN(x[1]);
            } else if (ph == 5) {
                x[0] += ga * x[4]; PIN(x[0]); x[1] += gb * x[5]; PIN(x[1]);
                P = pk2(x[0], x[1]);
            } else if (ph == 6) {
                const u32 Q = (u32)__builtin_amdgcn_update_dpp(0, (int)P, 0xB1, 0xf, 0xf, true);
                __builtin_amdgcn_raw_buffer_store_b32(__builtin_amdgcn_perm(P, Q, sel), ors, oo, be * (256 * DM * 2), 0);
            }
        };
#pragma unroll
        for (int q = -3; q <= QRES; ++q) Br[q + 3] = ldB(q);
#pragma unroll
        for (int q = -3; q <= QRES; ++q) asm volatile("" : "+v"(Br[q + 3]));
        asm volatile("" : "+v"(yb), "+v"(ym), "+v"(yc), "+v"(xr), "+v"(bb), "+v"(gl) : "v"(oo), "v"(sel), "v"(cmask) : "memory");
        STAMP(7);
        __builtin_amdgcn_s_barrier();
        asm volatile("" ::: "memory");
        STAMP(8);
#pragma unroll
        for (int i = 0; i < 4; ++i) { ldA(i, 0); ldA(i, 1); }
        sfor<0, 8>([&](auto BE) __attribute__((always_inline)) {
            constexpr int be = BE.v;
            constexpr int ns = 4 * be > QRES ? 4 * be - QRES : 0;
            sfor<0, 4 * be + 4>([&](auto S) __attribute__((always_inline)) {
                constexpr int st = S.v;
                constexpr int i = st < 4 * be - ns ? st + ns : st < 4 * be ? st - (4 * be - ns) : st;
                constexpr int q = 4 * be - i;
                const u4v bfr = (q > QRES) ? Bs[i & 7] : Br[q > QRES ? 0 : q + 3];
                if (st == 0) acc[be & 1] = __builtin_amdgcn_mfma_f32_32x32x16_bf16(__builtin_bit_cast(bf8, A[i]), __builtin_bit_cast(bf8, bfr), f16v{}, 0, 0, 0);
                else acc[be & 1] = __builtin_amdgcn_mfma_f32_32x32x16_bf16(__builtin_bit_cast(bf8, A[i]), __builtin_bit_cast(bf8, bfr), acc[be & 1], 0, 0, 0);
                __builtin_amdgcn_sched_barrier(0);
                if (be == 0) { ldA(4 + st, 0); ldA(4 + st, 1); }
                if (be == 1) ldA(8 + (st >> 1), st & 1);
                if (be == 2 && st >= 8) { ldA(12 + st - 8, 0); ldA(12 + st - 8, 1); }
                if (be == 3 && st >= 8) ldA(16 + ((st - 8) >> 1), st & 1);
                if (be == 4 && st >= 12) ldA(20 + ((st - 12) >> 1), st & 1);
                if (be == 6 && st >= 2 && st < 10) ldA(24 + ((st - 2) >> 1), st & 1);
                if (be == 7 && st >= 2 && st < 10) ldA(28 + ((st - 2) >> 1), st & 1);
                {
                    constexpr int i2 = st + PD - (4 * be - ns);
                    if (i2 >= 0 && i2 < ns) Bs[i2 & 7] = ldB(4 * be - i2);
                }
                if (be > 1 && st < 2) {
                    __builtin_amdgcn_s_setprio(0); __builtin_amdgcn_s_setprio(0); __builtin_amdgcn_s_setprio(0);
                    if (be != 3 && be != 4 && be != 5 && be != 6) { __builtin_amdgcn_s_setprio(0); __builtin_amdgcn_s_setprio(0); }
                }
                if (be == 1 && st < 2) { __builtin_amdgcn_s_setprio(0); __builtin_amdgcn_s_setprio(0); }
                if (be > 0 && st >= 2) post(be - 1, st - 2);
                constexpr int rb[7] = {3, 4, 5, 5, 6, 6, 7}, rs[7] = {0, 0, 0, 8, 0, 14, 14};
                constexpr int cbt[7] = {3, 4, 5, 5, 6, 6, 7}, cs[7] = {7, 8, 8, 16, 10, 20, 20};
#pragma unroll
                for (int m = 0; m < 7; ++m) {
                    if (be == rb[m] && st == rs[m]) rd(m, 0);
                    if (be == rb[m] && st == rs[m] + 1) rd(m, 1);
                    if (be == rb[m] && st == rs[m] + 2) rdu(m);
                }
                sfor<0, 7>([&](auto M) __attribute__((always_inline)) {
                    constexpr int m = M.v;
                    constexpr int r = st - cs[m];
                    if constexpr (be == cbt[m] && r >= 0 && r < 2) mix(m, r - 2);
                    if constexpr (be == cbt[m] && m == 0 && r >= 2 && r < 9) mix(m, r - 2);
                    if constexpr (be == cbt[m] && m > 0 && r >= 2 && r < 8) mix(m, r - 1);
                });
                if (be == 7 && st == 22) rdu(7);
                if (be == 7 && st == 28) coef(7, 0);
                if (be == 7 && st == 29) coef(7, 1);
                __builtin_amdgcn_sched_barrier(0);
            });
            if (be == 0) STAMP(9);
            if (be == 3) STAMP(10);
        });
        STAMP(11);
#pragma unroll
        for (int ph = 0; ph < 6; ++ph) post(7, ph);
        rd(7, 0); rd(7, 1);
        sfor<-2, 7>([&](auto PH) __attribute__((always_inline)) { mix(7, PH.v); });
        STAMP(12);
        STAMP_DUMP;
        return;
    }

    const int cw = w - 4;
    gu64* gp = (gu64*)(ws + WS_G) + cw * 256 + (l >> 1);
    u64 gg[8];
    {
        gu64* fp = (gu64*)(ws + WS_FLAG + ((bx * 4 + (bj >> 3)) * 4 + cw) * 128) + (l & 15);
        if (POLL_SLEEP0) __builtin_amdgcn_s_sleep(POLL_SLEEP0);
#pragma nounroll
        for (u32 spins = 0; spins < SPIN_CAP; ++spins) {
            if (__all(__hip_atomic_load(fp, RLX_AGENT) == tag)) break;
            __builtin_amdgcn_s_sleep(POLL_SLEEP);
        }
        asm volatile("" ::: "memory");
        STAMP(6);
        const u32 xo = (u32)(bx * 32 + 8 * cw + (l >> 5)) * 16384u + (u32)(bj * 512 + (l & 31) * 16);
        u4v xv[4];
#pragma unroll
        for (int pc = 0; pc < 4; ++pc) xv[pc] = __builtin_amdgcn_raw_buffer_load_b128(xrs, xo + 32768u * pc, 0, 16);
#pragma unroll
        for (int be = 0; be < 8; ++be) gg[be] = __hip_atomic_load(gp + 32 * be, RLX_AGENT);
        LAS char* xb = sm + XC_OFF + ((l >> 2) & 3) * XC_CONV + 2 * (2040 - 64 * (8 * cw + (l >> 5)) - 32 * ((l >> 4) & 1) - 8 * (l & 3));
#pragma unroll
        for (int pc = 0; pc < 4; ++pc) {
            LAS char* p = xb - 256 * pc;
            *(LAS u4v*)p = xv[pc];
            *(LAS u3v*)(p + XC_COPY) = u3v{__builtin_amdgcn_alignbit(xv[pc][1], xv[pc][0], 16), __builtin_amdgcn_alignbit(xv[pc][2], xv[pc][1], 16),
                                           __builtin_amdgcn_alignbit(xv[pc][3], xv[pc][2], 16)};
            *(LAS u16*)(p + XC_COPY + 12) = (u16)(xv[pc][3] >> 16);
            *(LAS u16*)(p + XC_COPY - 2) = (u16)xv[pc][0];
        }
    }
    STAMP(7);
    asm volatile("s_waitcnt lgkmcnt(0)" ::: "memory");
    __builtin_amdgcn_s_barrier();
    STAMP(8);

    {
        if (HPRIO) __builtin_amdgcn_s_setprio(HPRIO);
#pragma nounroll
        for (u32 spins = 0; spins < SPIN_CAP; ++spins) {
            u32 bad = 0;
#pragma unroll
            for (int be = 0; be < 8; ++be) bad |= (u32)(gg[be] >> 32) ^ (u32)tag;
            if (__all(bad == 0)) break;
            __builtin_amdgcn_s_sleep(4);
#pragma unroll
            for (int be = 0; be < 8; ++be) gg[be] = __hip_atomic_load(gp + 32 * be, RLX_AGENT);
        }
        STAMP(9);
#pragma unroll
        for (int be = 0; be < 8; ++be) *(LAS u32*)(sm + TAB_OFF + 1024 * be + 256 * cw + 4 * l) = (u32)gg[be];
        asm volatile("" ::: "memory");
        if (l == 0) *(volatile LAS unsigned char*)(sm + FL_OFF + 4 + cw) = 0xff;
        if (blockIdx.x == 0) {
            if (l == 0) {
                const u32 old = __hip_atomic_fetch_add((LAS u32*)(sm + FL_OFF + 64), 1u, __ATOMIC_RELAXED, __HIP_MEMORY_SCOPE_WORKGROUP);
                if (old == 3u) __hip_atomic_store((gu64*)ws, tag, RLX_AGENT);
            }
        }
        STAMP(10);
    }
    STAMP_DUMP;
}

extern "C" void kernel_launch(void* const* d_in, const int* in_sizes, int n_in,
                              void* d_out, int out_size, void* d_ws, size_t ws_size,
                              hipStream_t stream) {
    fused_k<<<256, 512, 0, stream>>>((const u16*)d_in[0], (const u16*)d_in[1], (const u16*)d_in[2], (const u16*)d_in[3], (const u16*)d_in[4],
             (const u16*)d_in[5], (const u16*)d_in[6], (const u16*)d_in[8], (const u16*)d_in[9], (const u16*)d_in[10],
             (const u16*)d_in[11], (const u16*)d_in[12], (const u16*)d_in[13], (u16*)d_out, (char*)d_ws);
}
```

```cpp
#include <hip/hip_runtime.h>
#include <stdint.h>

typedef unsigned short u16;
typedef unsigned int u32;
typedef unsigned long long u64;
typedef __attribute__((ext_vector_type(2))) __bf16 bf2;
typedef __attribute__((ext_vector_type(8))) __bf16 bf8;
typedef __attribute__((ext_vector_type(4))) short s4v;
typedef __attribute__((ext_vector_type(2))) u32 u2v;
typedef __attribute__((ext_vector_type(3))) u32 u3v;
typedef __attribute__((ext_vector_type(4))) u32 u4v;
typedef __attribute__((ext_vector_type(2))) float f2v;
typedef __attribute__((ext_vector_type(4))) float f4v;
typedef __attribute__((ext_vector_type(16))) float f16v;
#define GAS __attribute__((address_space(1)))
#define LAS __attribute__((address_space(3)))
typedef GAS u64 gu64;
#define RLX_AGENT __ATOMIC_RELAXED, __HIP_MEMORY_SCOPE_AGENT

#define STAMP_DECL
#define STAMP(i)
#define STAMP_DUMP
#define NT " nt"
#define POLL_SLEEP0 4
#define POLL_SLEEP 2
#define CODE_PF 8
#define BSPLIT 1
#define HPRIO 0
#define GSCOPE __HIP_MEMORY_SCOPE_AGENT
#define CODE_BYTES 15360
#define QRES 13
#define RS0 3
#define GEARLY 1
#define POLL_ATOMIC 1
#define GPOS 0
#define TWOSTEP 1
#define B2STEP 5
#define WPRE 0
#define GSLEEP 8
#define P1PRIO 1
#define WBL2 0
#define TOUCH 1

constexpr int SEQ = 2048, DM = 512, NS = 128;

constexpr int XC_OFF = 0, XC_CONV = 8384, XC_COPY = 4160;
constexpr int Z_OFF = 33536, Z_CONV = 4608, Z_PAD = 512;
constexpr int Y_OFF = 51968, Y_CONV = 8256;
constexpr int TAB_OFF = 84992, TAB_CH = 32768;
constexpr int FL_OFF = 150528;
constexpr int LDS_BYTES = 150912;
constexpr int TU_OFF = Y_OFF, TV_OFF = Y_OFF + 8192, TK_OFF = Y_OFF + 16384;
constexpr int WS_FLAG = 16384, WS_G = 65536, WS_X = 1 << 20;
constexpr u32 SPIN_CAP = 1u << 17;

__device__ __forceinline__ float bf2f(u32 x) { return __uint_as_float(x << 16); }
__device__ __forceinline__ float bfhi(u32 x) { return __uint_as_float(x & 0xffff0000u); }
__device__ __forceinline__ u32 pk2(float a, float b) { bf2 r; r[0] = (__bf16)a; r[1] = (__bf16)b; return __builtin_bit_cast(u32, r); }
__device__ __forceinline__ float rbf(float a) { return __uint_as_float(pk2(0.f, a)); }
__device__ __forceinline__ float dot2(u32 a, u32 b, float c) {
    return __builtin_amdgcn_fdot2_f32_bf16(__builtin_bit_cast(bf2, a), __builtin_bit_cast(bf2, b), c, false);
}
struct cmult { f2v b, bs; };
__device__ __forceinline__ cmult mkm(f2v b) { return cmult{b, f2v{-b[1], b[0]}}; }
__device__ __forceinline__ f2v cmul(f2v a, const cmult& m) {
    return __builtin_elementwise_fma(f2v{a[0], a[0]}, m.b, f2v{a[1], a[1]} * m.bs);
}
__device__ __forceinline__ f2v cexp_p(float lz, float rev, float p) {
    const float m = __builtin_amdgcn_exp2f(lz * p);
    const float a = __builtin_amdgcn_fractf(rev * p);
    return f2v{m * __builtin_amdgcn_cosf(a), m * __builtin_amdgcn_sinf(a)};
}
template <int CTRL> __device__ __forceinline__ float dppz(float v) {
    return __int_as_float(__builtin_amdgcn_update_dpp(0, __float_as_int(v), CTRL, 0xf, 0xf, true));
}
template <int I> struct ic { static constexpr int v = I; };
template <int B, int E, class F> __device__ __forceinline__ void sfor(F&& f) {
    if constexpr (B < E) { f(ic<B>{}); sfor<B + 1, E>(f); }
}
__device__ __forceinline__ void dma16(const void* base, u32 off, u32 lds) {
    u32 keep;
    asm volatile("s_mov_b32 %0, m0\n\ts_mov_b32 m0, %3\n\ts_nop 0\n\tglobal_load_lds_dwordx4 %1, %2" NT "\n\ts_mov_b32 m0, %0"
                 : "=&s"(keep) : "v"(off), "s"(base), "s"(lds) : "memory");
}

__global__ void __launch_bounds__(512, 2)
fused_k(const u16* __restrict__ gu, const u16* __restrict__ glar, const u16* __restrict__ gaim,
        const u16* __restrict__ gcre, const u16* __restrict__ gcim, const u16* __restrict__ gldt,
        const u16* __restrict__ gdsk, const u16* __restrict__ gk, const u16* __restrict__ gv,
        const u16* __restrict__ gshk, const u16* __restrict__ gssk, const u16* __restrict__ gssd,
        const u16* __restrict__ ggw, u16* __restrict__ gout, char* ws)
{
    __shared__ __attribute__((aligned(16))) char smem[LDS_BYTES];
    LAS char* sm = (LAS char*)smem;
    const u32 smb = (u32)(size_t)sm;
    const int tid = threadIdx.x, l = tid & 63;
    const int w = __builtin_amdgcn_readfirstlane(tid >> 6);
    const int bx = blockIdx.x & 7, bj = blockIdx.x >> 3;
    const int d0 = 64 * bx + 2 * bj;
    STAMP_DECL;
    STAMP(0);
    if (w == 7) {
        u64 pc; u32 keep;
        asm volatile("s_getpc_b64 %0\n\ts_mov_b32 %1, m0\n\ts_mov_b32 m0, %3\n\ts_nop 0\n\tglobal_load_lds_dword %2, %0\n\ts_mov_b32 m0, %1"
                     : "=&s"(pc), "=&s"(keep) : "v"(min((u32)((bj * CODE_PF + (l & (CODE_PF - 1))) & 127) * 128u, (u32)CODE_BYTES)), "s"(smb + FL_OFF + 128) : "memory");
    }
    u32 go, po, so_;
    {
        const int r = 8 * w + (l >> 3);
        const int f = ((r >> 1) & 1) | (((r >> 3) & 1) << 1);
        go = (u32)(64 * bj + r) * (DM * 2) + 128 * bx + 16 * ((l & 7) ^ (f << 1));
        po = 2u * (u32)((d0 + (w & 1)) * NS + 64 * ((w >> 1) & 1) + l);
        so_ = 8u * (u32)(64 * bx + 16 * (w & 3) + (l & 15));
        asm volatile("" : "+v"(go), "+v"(po), "+v"(so_));
    }
    asm volatile("" :: "s"(gu), "s"(glar), "s"(gaim), "s"(gcre), "s"(gcim), "s"(gldt), "s"(gdsk), "s"(gk), "s"(gv), "s"(gshk), "s"(gssk), "s"(gssd), "s"(ggw), "s"(gout), "s"(ws));
    const __amdgpu_buffer_rsrc_t xrs = __builtin_amdgcn_make_buffer_rsrc(ws + WS_X, 0, DM * 2 * 4096, 0x00020000);

    u32 p_lar, p_aim, p_cre, p_cim;
    if (w < 4) {
        asm volatile("global_load_ushort %0, %4, %5" NT "\n\tglobal_load_ushort %1, %4, %6" NT "\n\t"
                     "global_load_ushort %2, %4, %7" NT "\n\tglobal_load_ushort %3, %4, %8" NT
                     : "=&v"(p_lar), "=&v"(p_aim), "=&v"(p_cre), "=&v"(p_cim)
                     : "v"(po), "s"(glar), "s"(gaim), "s"(gcre), "s"(gcim) : "memory");
    } else if (w == 5 && bj > 0) {
        dma16(gk, go - 48 * DM * 2, smb + TK_OFF);
    }
    dma16(gk, go, smb + TK_OFF + 1024 + 1024 * w);
    dma16(gv, go, smb + TV_OFF + 1024 * w);
    dma16(gu, go, smb + TU_OFF + 1024 * w);
    auto late = [&](u2v& shk, u64& tag) __attribute__((always_inline)) {
        asm volatile("global_load_dwordx2 %0, %2, %3\n\ts_load_dwordx2 %1, %4, 0x0" : "=&v"(shk), "=&s"(tag) : "v"(so_), "s"(gshk), "s"(ws) : "memory");
    };

    const int cg = w & 3, th = w >> 2;
    auto phase1 = [&](u2v& shk, u64& tag, bool mwave) __attribute__((always_inline)) {
        const int G = l >> 4, q = (l >> 2) & 3, p = l & 3;
        const int row = 32 * th + 8 * G + 7 - q;
        const int f = ((row >> 1) & 1) | (((row >> 3) & 1) << 1);
        u32 a0 = smb + TU_OFF - 512 + row * 128 + ((cg ^ f) << 5) + 8 * p;
        u32 a2 = smb + TK_OFF + row * 128 + ((cg ^ f ^ 2) << 5) + 8 * p;
        u32 xo = (u32)(bx * 32 + bj) * 16384u + (u32)((8 * cg + ((l & 15) >> 1)) * 512 + th * 256 + (l & 1) * 64 + G * 16);
        float ev[8] = {0.f, 0.f, 0.f, 0.f, 0.f, 0.f, 0.f, 0.f};
        asm volatile("" : "+v"(a0), "+v"(a2), "+v"(xo), "+v"(ev[0]), "+v"(ev[1]), "+v"(ev[2]), "+v"(ev[3]), "+v"(ev[4]), "+v"(ev[5]), "+v"(ev[6]), "+v"(ev[7]));
        STAMP(2);
        if (mwave) asm volatile("s_waitcnt vmcnt(0) lgkmcnt(0)" : "+v"(shk), "+s"(tag) :: "memory");
        else asm volatile("s_waitcnt vmcnt(3) lgkmcnt(0)" : "+v"(shk), "+s"(tag) :: "memory");
        tag += 1ull;
        if (P1PRIO) __builtin_amdgcn_s_setprio(3);
        __builtin_amdgcn_s_barrier();
        STAMP(3);
        typedef LAS s4v* trp;
        auto tr = [&](u32 a) __attribute__((always_inline)) { return __builtin_bit_cast(u2v, __builtin_amdgcn_ds_read_tr16_b64_v4i16((trp)(size_t)a)); };
        const u2v ka = tr(a0 + (TK_OFF - TU_OFF) + 1024 + 512), kb = tr(a0 + (TK_OFF - TU_OFF) + 1024), kc = tr(a2);
        const u2v ua = tr(a0 + 512), ub = tr(a0);
        const u2v va = tr(a0 + (TV_OFF - TU_OFF) + 512), vb = tr(a0 + (TV_OFF - TU_OFF));
        __builtin_amdgcn_sched_barrier(0);
        const u32 K[6] = {ka[0], ka[1], kb[0], kb[1], kc[0], kc[1]};
        const u32 V[4] = {va[0], va[1], vb[0], vb[1]};
        u32 Ks[5];
#pragma unroll
        for (int n = 0; n < 5; ++n) Ks[n] = __builtin_amdgcn_alignbit(K[n + 1], K[n], 16);
#pragma unroll
        for (int n = 0; n < 4; ++n) { ev[2 * n] = dot2(K[n], shk[0], ev[2 * n]); ev[2 * n + 1] = dot2(Ks[n], shk[0], ev[2 * n + 1]); }
        u4v ud{ua[0], ua[1], ub[0], ub[1]};
        __builtin_amdgcn_raw_buffer_store_b128(ud, xrs, xo, 0, 16);
        __builtin_amdgcn_sched_barrier(0);
#pragma unroll
        for (int n = 0; n < 4; ++n) { ev[2 * n] = dot2(K[n + 1], shk[1], ev[2 * n]); ev[2 * n + 1] = dot2(Ks[n + 1], shk[1], ev[2 * n + 1]); }
        u4v kv;
#pragma unroll
        for (int n = 0; n < 4; ++n) kv[n] = pk2(ev[2 * n] * bf2f(V[n]), ev[2 * n + 1] * bfhi(V[n]));
        __builtin_amdgcn_raw_buffer_store_b128(kv, xrs, xo + 128u, 0, 16);
        asm volatile("" :: "v"(ud));
        __builtin_amdgcn_sched_barrier(0);
        if (P1PRIO) __builtin_amdgcn_s_setprio(0);
        STAMP(4);
    };
    auto signal = [&](u64 tag) __attribute__((always_inline)) {
        if (l == 0) __hip_atomic_store((gu64*)(ws + WS_FLAG + ((bx * 4 + cg) * 4 + ((bj >> 1) & 3)) * 128) + ((bj >> 3) * 2 + (bj & 1)) * 2 + th, tag, RLX_AGENT);
        STAMP(5);
    };

    if (__builtin_expect(w >= 4, 0)) {
    const int cw = w - 4;
    if (HPRIO) __builtin_amdgcn_s_setprio(HPRIO);
    u64 tag; u2v shk;
    {
        const u4v z4{0, 0, 0, 0};
        const int zo = (l < 32) ? Z_OFF + cw * Z_CONV + 16 * l
                                : (l < 35) ? XC_OFF + cw * XC_CONV + 4096 + (l == 32 ? 0 : XC_COPY - 16 * (l - 33)) : FL_OFF + 16 * ((l - 35) & 7);
        *(LAS u4v*)(sm + zo) = z4;
        *(LAS u4v*)(sm + ((w == 5 && bj == 0) ? TK_OFF + 16 * l : zo)) = z4;
    }
    late(shk, tag);
    if (GSLEEP) __builtin_amdgcn_s_sleep(GSLEEP);
    u4v gr0, gr1, gwv;
    {
        const u32 ro = (u32)(8 * blockIdx.x + 2 * cw) * (DM * 2) + 16 * l;
        asm volatile("global_load_dwordx4 %0, %3, %5" NT "\n\tglobal_load_dwordx4 %1, %3, %5 offset:1024" NT "\n\tglobal_load_dwordx4 %2, %4, %6"
                     : "=&v"(gr0), "=&v"(gr1), "=&v"(gwv) : "v"(ro), "v"(16u * l), "s"(gu), "s"(ggw) : "memory");
    }
    STAMP(1);
    phase1(shk, tag, false);
    {
        asm volatile("s_waitcnt vmcnt(2)" : "+v"(gr0), "+v"(gr1), "+v"(gwv) :: "memory");
        const float s0 = dot2(gr0[0], gwv[0], dot2(gr0[1], gwv[1], dot2(gr0[2], gwv[2], dot2(gr0[3], gwv[3], 0.f))));
        const float s1 = dot2(gr1[0], gwv[0], dot2(gr1[1], gwv[1], dot2(gr1[2], gwv[2], dot2(gr1[3], gwv[3], 0.f))));
        float ee = ((l & 1) ? s1 : s0) + dppz<0xB1>((l & 1) ? s0 : s1);
        ee += dppz<0x4E>(ee); ee += dppz<0x124>(ee); ee += dppz<0x128>(ee);
        const float r0 = __int_as_float(__builtin_amdgcn_readlane(__float_as_int(ee), 0)) + __int_as_float(__builtin_amdgcn_readlane(__float_as_int(ee), 16))
                       + __int_as_float(__builtin_amdgcn_readlane(__float_as_int(ee), 32)) + __int_as_float(__builtin_amdgcn_readlane(__float_as_int(ee), 48));
        const float r1 = __int_as_float(__builtin_amdgcn_readlane(__float_as_int(ee), 1)) + __int_as_float(__builtin_amdgcn_readlane(__float_as_int(ee), 17))
                       + __int_as_float(__builtin_amdgcn_readlane(__float_as_int(ee), 33)) + __int_as_float(__builtin_amdgcn_readlane(__float_as_int(ee), 49));
        const float g0 = __builtin_amdgcn_rcpf(1.f + __builtin_amdgcn_exp2f(-1.44269504088896341f * rbf(r0)));
        const float g1 = __builtin_amdgcn_rcpf(1.f + __builtin_amdgcn_exp2f(-1.44269504088896341f * rbf(r1)));
        const int row = 8 * blockIdx.x + 2 * cw;
        const int be = row >> 8, cc = (row >> 3) & 31, hh = (row >> 2) & 1;
        const int gi = (((cc >> 4) + 2 * hh) * 8 + be) * 32 + (cc & 15) * 2 + (cw & 1);
        if (l == 0) __hip_atomic_store((gu64*)(ws + WS_G) + gi, (tag << 32) | pk2(g0, g1), RLX_AGENT);
        asm volatile("s_waitcnt vmcnt(1)" ::: "memory");
        signal(tag);
    }
    gu64* gp = (gu64*)(ws + WS_G) + cw * 256 + (l >> 1);
    u64 gg[8];
    {
        gu64* fp = (gu64*)(ws + WS_FLAG + ((bx * 4 + (bj >> 3)) * 4 + cw) * 128) + (l & 15);
        if (POLL_SLEEP0) __builtin_amdgcn_s_sleep(POLL_SLEEP0);
        if (l < 16) {
            const u64 zero = 0;
#pragma nounroll
            for (u32 spins = 0; spins < SPIN_CAP; ++spins) {
                u64 v;
                asm volatile("global_atomic_add_x2 %0, %1, %2, off sc0 sc1\n\ts_waitcnt vmcnt(0)" : "=&v"(v) : "v"(fp), "v"(zero) : "memory");
                if (__all(v == tag)) break;
                __builtin_amdgcn_s_sleep(POLL_SLEEP);
            }
        }
        asm volatile("" ::: "memory");
        STAMP(6);
        const u32 xo = (u32)(bx * 32 + 2 * cw + (l >> 5)) * 16384u + (u32)(bj * 512 + (l & 31) * 16);
        u4v xv[4];
#pragma unroll
        for (int pc = 0; pc < 4; ++pc) xv[pc] = __builtin_amdgcn_raw_buffer_load_b128(xrs, xo + 131072u * pc, 0, 16);
#pragma unroll
        for (int be = 0; be < 8; ++be) gg[be] = __hip_atomic_load(gp + 32 * be, RLX_AGENT);
        LAS char* xb = sm + XC_OFF + ((l >> 2) & 3) * XC_CONV + 2 * (2040 - 64 * (2 * cw + (l >> 5)) - 32 * ((l >> 4) & 1) - 8 * (l & 3));
#pragma unroll
        for (int pc = 0; pc < 4; ++pc) {
            LAS char* p = xb - 1024 * pc;
            *(LAS u4v*)p = xv[pc];
            *(LAS u3v*)(p + XC_COPY) = u3v{__builtin_amdgcn_alignbit(xv[pc][1], xv[pc][0], 16), __builtin_amdgcn_alignbit(xv[pc][2], xv[pc][1], 16),
                                           __builtin_amdgcn_alignbit(xv[pc][3], xv[pc][2], 16)};
            *(LAS u16*)(p + XC_COPY + 12) = (u16)(xv[pc][3] >> 16);
            *(LAS u16*)(p + XC_COPY - 2) = (u16)xv[pc][0];
            if (pc == 0) {
                asm volatile("s_waitcnt lgkmcnt(0)" ::: "memory");
                __builtin_amdgcn_s_barrier();
                asm volatile("" ::: "memory");
            }
        }
    }
    STAMP(7);
    asm volatile("s_waitcnt lgkmcnt(0)" ::: "memory");
    __builtin_amdgcn_s_barrier();
    STAMP(8);

    {
#pragma nounroll
        for (u32 spins = 0; spins < SPIN_CAP; ++spins) {
            u32 bad = 0;
#pragma unroll
            for (int be = 0; be < 8; ++be) bad |= (u32)(gg[be] >> 32) ^ (u32)tag;
            if (__all(bad == 0)) break;
            __builtin_amdgcn_s_sleep(4);
#pragma unroll
            for (int be = 0; be < 8; ++be) gg[be] = __hip_atomic_load(gp + 32 * be, RLX_AGENT);
        }
        STAMP(9);
#pragma unroll
        for (int be = 0; be < 8; ++be) *(LAS u32*)(sm + TAB_OFF + 1024 * be + 256 * cw + 4 * l) = (u32)gg[be];
        asm volatile("" ::: "memory");
        if (l == 0) *(volatile LAS unsigned char*)(sm + FL_OFF + 4 + cw) = 0xff;
        if (blockIdx.x == 0) {
            if (l == 0) {
                const u32 old = __hip_atomic_fetch_add((LAS u32*)(sm + FL_OFF + 64), 1u, __ATOMIC_RELAXED, __HIP_MEMORY_SCOPE_WORKGROUP);
                if (old == 3u) __hip_atomic_store((gu64*)ws, tag, RLX_AGENT);
            }
        }
        STAMP(10);
        if (WBL2 && bj == 0 && cw == 0) {
#pragma nounroll
            for (u32 spins = 0; spins < SPIN_CAP; ++spins) { if ((*(volatile LAS u32*)(sm + FL_OFF) & 0xffu) >= WBL2) break; __builtin_amdgcn_s_sleep(2); }
            asm volatile("buffer_wbl2 sc1" ::: "memory");
        }
    }
    STAMP_DUMP;
    return;
    }

    {
        u64 tag; u2v shk; u32 p_ldt, skip;
        asm volatile("s_load_dword %0, %2, %4\n\ts_load_dword %1, %3, %4"
                     : "=&s"(p_ldt), "=&s"(skip) : "s"(gldt), "s"((w & 2) ? gssd : gdsk), "s"(2 * d0) : "memory");
        late(shk, tag);
        asm volatile("s_waitcnt vmcnt(4) lgkmcnt(0)" : "+v"(p_lar), "+v"(p_aim), "+v"(p_cre), "+v"(p_cim), "+s"(p_ldt), "+s"(skip) :: "memory");
        p_ldt = (p_ldt >> (16 * (w & 1))) & 0xffffu;
        skip = (skip >> (16 * (w & 1))) & 0xffffu;
        STAMP(1);
        u4v ta_[8], tb_[8];
        const float dt = rbf(expf(bf2f(p_ldt)));
        const float zr = -rbf(expf(bf2f(p_lar))) * dt;
        const float zi = bf2f(p_aim) * dt;
        const float lz = zr * 1.44269504088896341f, rev = zi * 0.15915494309189535f;
        const f2v e8 = cexp_p(lz, rev, 8.f);
        const cmult E8 = mkm(e8);
        {
            const cmult E1 = mkm(cexp_p(lz, rev, 1.f)), E4 = mkm(cexp_p(lz, rev, 4.f)), E256 = mkm(cexp_p(lz, rev, 256.f));
            f2v a0[16], a1[16];
            a0[0] = f2v{2.f * (bf2f(p_cre) * dt), 2.f * (bf2f(p_cim) * dt)};
            a0[4] = cmul(a0[0], E4);
#pragma unroll
            for (int g = 1; g < 4; ++g) { a0[g] = cmul(a0[g - 1], E1); a0[g + 4] = cmul(a0[g + 3], E1); }
            a0[8] = cmul(a0[0], E256); a0[9] = cmul(a0[2], E256); a0[10] = cmul(a0[4], E256); a0[11] = cmul(a0[6], E256);
            a0[12] = cmul(a0[8], E8);
            a0[13] = cmul(a0[8], E256); a0[14] = cmul(a0[10], E256); a0[15] = cmul(a0[12], E256);
            a1[0] = cmul(a0[13], E256); a1[1] = cmul(a0[14], E256); a1[2] = cmul(a0[15], E256);
            a1[3] = cmul(a1[0], E256); a1[4] = cmul(a1[2], E256);
            a1[5] = cmul(a1[3], E256); a1[6] = cmul(a1[4], E256);
            a1[7] = cmul(a1[5], E256); a1[8] = cmul(a1[6], E256);
            a1[9] = cmul(a1[7], E256); a1[10] = cmul(a1[8], E256);
#pragma unroll
            for (int g = 11; g < 16; ++g) a1[g] = f2v{0.f, 0.f};
#pragma unroll
            for (int cc = 0; cc < 4; ++cc) {
                const int g = 4 * cc;
                ta_[2 * cc] = u4v{pk2(a0[g][0], a0[g + 1][0]), pk2(a0[g + 2][0], a0[g + 3][0]), pk2(a1[g][0], a1[g + 1][0]), pk2(a1[g + 2][0], a1[g + 3][0])};
                ta_[2 * cc + 1] = u4v{pk2(-a0[g][1], -a0[g + 1][1]), pk2(-a0[g + 2][1], -a0[g + 3][1]), pk2(-a1[g][1], -a1[g + 1][1]), pk2(-a1[g + 2][1], -a1[g + 3][1])};
                asm volatile("" : "+v"(ta_[2 * cc]), "+v"(ta_[2 * cc + 1]));
            }
        }
        const cmult E64 = mkm(cexp_p(lz, rev, 64.f));
        f2v e[8];
        e[0] = f2v{1.f, 0.f}; e[1] = e8;
        e[4] = cexp_p(lz, rev, 32.f);
        e[2] = cmul(e[1], E8); e[5] = cmul(e[4], E8); e[3] = cmul(e[2], E8);
        e[6] = cmul(e[5], E8); e[7] = cmul(e[6], E8);
        auto btab = [&](int cc) __attribute__((always_inline)) {
            if (cc > 0) {
#pragma unroll
                for (int g = 0; g < 8; ++g) e[g] = cmul(e[g], E64);
            }
            tb_[2 * cc] = u4v{pk2(e[0][0], e[1][0]), pk2(e[2][0], e[3][0]), pk2(e[4][0], e[5][0]), pk2(e[6][0], e[7][0])};
            tb_[2 * cc + 1] = u4v{pk2(e[0][1], e[1][1]), pk2(e[2][1], e[3][1]), pk2(e[4][1], e[5][1]), pk2(e[6][1], e[7][1])};
            asm volatile("" : "+v"(tb_[2 * cc]), "+v"(tb_[2 * cc + 1]));
        };
#pragma unroll
        for (int cc = 0; cc < BSPLIT; ++cc) btab(cc);
        phase1(shk, tag, true);
        LAS char* tab = sm + TAB_OFF + w * 16384;
        {
            LAS char* tw = tab + l * 64;
            const int sw = (l >> 1) & 3;
#pragma unroll
            for (int cc = BSPLIT; cc < 4; ++cc) btab(cc);
#pragma unroll
            for (int cc = 0; cc < WPRE; ++cc) { *(LAS u4v*)(tw + ((cc ^ sw) << 4)) = ta_[2 * cc]; *(LAS u4v*)(tw + 4096 + ((cc ^ sw) << 4)) = ta_[2 * cc + 1]; }
            asm volatile("s_waitcnt vmcnt(0)" ::: "memory");
            signal(tag);
            if (w >= 2) {
                const u32 so = (u32)(d0 + (w & 1)) * (SEQ * 2) + 16 * l;
                const u32 zd = smb + Z_OFF + w * Z_CONV + Z_PAD;
#pragma unroll
                for (int pc = 0; pc < 4; ++pc) dma16(gssk, so + 1024 * pc, zd + 1024 * pc);
            }
#pragma unroll
            for (int cc = WPRE; cc < 4; ++cc) { *(LAS u4v*)(tw + ((cc ^ sw) << 4)) = ta_[2 * cc]; *(LAS u4v*)(tw + 4096 + ((cc ^ sw) << 4)) = ta_[2 * cc + 1]; }
#pragma unroll
            for (int cc = 0; cc < 4; ++cc) { *(LAS u4v*)(tw + 8192 + ((cc ^ sw) << 4)) = tb_[2 * cc]; *(LAS u4v*)(tw + 12288 + ((cc ^ sw) << 4)) = tb_[2 * cc + 1]; }
        }
        {
            f16v kd = {};
            const int h = l >> 5, blk = (l >> 4) & 1, q = (l >> 2) & 3, p = l & 3;
            typedef LAS s4v* trp;
            const int b0 = (8 * h + q) * 64 + (((2 * blk + (p >> 1)) ^ (q >> 1)) << 4) + 8 * (p & 1);
            const int b1 = (8 * h + 4 + q) * 64 + (((2 * blk + (p >> 1)) ^ (2 + (q >> 1))) << 4) + 8 * (p & 1);
            u4v fa[8], fb[8];
#pragma unroll
            for (int s = 0; s < 8; ++s) {
                u2v x0 = __builtin_bit_cast(u2v, __builtin_amdgcn_ds_read_tr16_b64_v4i16((trp)(tab + s * 1024 + b0)));
                u2v x1 = __builtin_bit_cast(u2v, __builtin_amdgcn_ds_read_tr16_b64_v4i16((trp)(tab + s * 1024 + b1)));
                u2v y0 = __builtin_bit_cast(u2v, __builtin_amdgcn_ds_read_tr16_b64_v4i16((trp)(tab + 8192 + s * 1024 + b0)));
                u2v y1 = __builtin_bit_cast(u2v, __builtin_amdgcn_ds_read_tr16_b64_v4i16((trp)(tab + 8192 + s * 1024 + b1)));
                fa[s] = u4v{x0[0], x0[1], x1[0], x1[1]}; fb[s] = u4v{y0[0], y0[1], y1[0], y1[1]};
                if (s >= 2) kd = __builtin_amdgcn_mfma_f32_32x32x16_bf16(__builtin_bit_cast(bf8, fa[s - 2]), __builtin_bit_cast(bf8, fb[s - 2]), kd, 0, 0, 0);
                __builtin_amdgcn_sched_barrier(0);
            }
#pragma unroll
            for (int s = 6; s < 8; ++s) kd = __builtin_amdgcn_mfma_f32_32x32x16_bf16(__builtin_bit_cast(bf8, fa[s]), __builtin_bit_cast(bf8, fb[s]), kd, 0, 0, 0);
            volatile LAS u32* pairf = (volatile LAS u32*)(sm + FL_OFF + 16 + 4 * (w & 1));
            if (w >= 2) {
#pragma unroll
                for (int g = 0; g < 4; ++g) *(LAS f4v*)(tab + g * 1024 + l * 16) = f4v{kd[4 * g], kd[4 * g + 1], kd[4 * g + 2], kd[4 * g + 3]};
                asm volatile("" ::: "memory");
                if (l == 0) *pairf = 1u;
                asm volatile("s_waitcnt vmcnt(0)" ::: "memory");
                LAS u16* z0 = (LAS u16*)(sm + Z_OFF + w * Z_CONV + Z_PAD);
                if (l == 0) *z0 = (u16)pk2(bf2f(*z0) + bf2f(skip), 0.f);
            } else {
#pragma nounroll
                for (u32 spins = 0; spins < SPIN_CAP; ++spins) if (*pairf != 0u) break;
                asm volatile("" ::: "memory");
#pragma unroll
                for (int g = 0; g < 4; ++g) {
                    const f4v o = *(LAS f4v*)(tab + 32768 + g * 1024 + l * 16);
                    kd[4 * g] += o[0]; kd[4 * g + 1] += o[1]; kd[4 * g + 2] += o[2]; kd[4 * g + 3] += o[3];
                }
                if (l == 0) kd[0] += bf2f(skip);
                LAS char* zb = sm + Z_OFF + w * Z_CONV + Z_PAD + 16 * (l & 31);
                if (l < 32) {
                    *(LAS u4v*)(zb) = u4v{pk2(kd[0], kd[1]), pk2(kd[2], kd[3]), pk2(kd[4], kd[5]), pk2(kd[6], kd[7])};
                    *(LAS u4v*)(zb + 512) = u4v{pk2(kd[8], kd[8]), pk2(kd[9], kd[10]), pk2(kd[10], kd[10]), pk2(kd[11], kd[12])};
                    *(LAS u4v*)(zb + 1024) = u4v{pk2(kd[13], kd[13]), pk2(kd[13], kd[14]), pk2(kd[14], kd[14]), pk2(kd[15], kd[15])};
                } else {
                    *(LAS u4v*)(zb + 1536) = u4v{pk2(kd[0], kd[0]), pk2(kd[0], kd[1]), pk2(kd[1], kd[1]), pk2(kd[2], kd[2])};
#pragma unroll
                    for (int a = 0; a < 4; ++a) {
                        const float lo = kd[3 + 2 * a], hi = kd[4 + 2 * a];
                        const u32 ll = pk2(lo, lo);
                        *(LAS u4v*)(zb + 2048 + 512 * a) = u4v{ll, ll, pk2((l & 1) ? hi : lo, hi), pk2(hi, hi)};
                    }
                }
            }
        }
        STAMP(6);

        u32 abase[4];
#pragma unroll
        for (int g = 0; g < 4; ++g) {
            abase[g] = smb + XC_OFF + w * XC_CONV + ((l & 1) ? 0 : XC_COPY) + 4 * (1023 - (l >> 1) - 256 * g - 224);
            asm volatile("" : "+v"(abase[g]));
        }
        const LAS char* bb = sm + Z_OFF + w * Z_CONV + Z_PAD + 16 * ((l & 31) - 4 * (l >> 5));
        LAS char* yb = sm + Y_OFF + w * Y_CONV + 8 * l;
        u32 done_ = smb + FL_OFF + (l == 0 ? w : 128 + 4 * l); asm volatile("" : "+v"(done_));
        volatile LAS unsigned char* done = (volatile LAS unsigned char*)(size_t)done_;
        const int chb = l & 1;
        const int ta = 8 * (16 * (w & 1) + (l >> 2)) + 4 * (w >> 1) + (l & 2);
        const u32 oo = (u32)(ta + chb) * (DM * 2) + 2 * d0;
        const __amdgpu_buffer_rsrc_t ors = __builtin_amdgcn_make_buffer_rsrc(gout, 0, SEQ * DM * 2, 0x00020000);
        const LAS char* ym = sm + Y_OFF + chb * Y_CONV + 8 * (16 * w + (l >> 2)) + 2 * (l & 2);
        const LAS char* yc = sm + Y_OFF + chb * Y_CONV + 512 + ((w & 1) ? 128 * (w - 1) : 128 * (w + 1) - 1024) + 8 * (7 + (l < 12 ? (l & 12) : 0)) + 2 * (l & 2);
        const LAS char* xr = sm + XC_OFF + chb * XC_CONV + 2 * (2046 - ta) - 512 * 7;
        const float cmask = l < 12 ? 1.f : 0.f;
        const u32 sel = chb ? 0x07060302u : 0x01000504u;
        const LAS char* gl = sm + TAB_OFF + 256 * w + 4 * l;
        constexpr int PD = 5;
        u32 flp_ = smb + FL_OFF; asm volatile("" : "+v"(flp_));
        const LAS char* flp = (const LAS char*)(size_t)flp_;
        u4v A[32], Br[QRES + 4], Bs[8];
        f16v acc[2];
        u32 G2[8], fh[8];
        u32 W0, W1, T0, T1, T2, T3;
        u32 fl[8], uu[8], ma[8], mb[8], ca[8], cb[8], P; float x[8], ga, gb, c1a, c1b;
#define PIN(v) asm("" : "+v"(v))
        auto ldA = [&](int i, int h) __attribute__((always_inline)) {
            const LAS u32* p = (const LAS u32*)(size_t)abase[i >> 3] + (224 - 32 * (i & 7)) + 2 * h;
            A[i][2 * h] = p[0]; A[i][2 * h + 1] = p[1];
        };
        auto ldB = [&](int q) __attribute__((always_inline)) { return *(const LAS u4v*)(bb + 128 * q); };
        auto horner = [&](int be, int n) __attribute__((always_inline)) {
            f16v& a = acc[be & 1];
            const int e = 8 - 4 * (n >> 2) + (n & 3);
            float t = a[e] + dppz<0x111>(a[e + 4]); PIN(t); a[e] = t;
        };
        auto post = [&](int be, int ph) __attribute__((always_inline)) {
            f16v& a = acc[be & 1];
            if (ph < 2) {
#pragma unroll
                for (int n = 5 * ph; n < 5 * ph + 5; ++n) horner(be, n);
            } else if (ph == 2) {
                horner(be, 10); horner(be, 11);
                W0 = pk2(a[12], a[13]); W1 = pk2(a[14], a[15]); T0 = pk2(a[8], a[9]); T1 = pk2(a[10], a[11]);
            } else if (ph == 3) {
                W0 = (u32)__builtin_amdgcn_update_dpp((int)W0, (int)T0, 0x104, 0xf, 0x4, false);
                W1 = (u32)__builtin_amdgcn_update_dpp((int)W1, (int)T1, 0x104, 0xf, 0x4, false);
                *(LAS u2v*)(yb + 1024 * be) = u2v{pk2(a[0], a[1]), pk2(a[2], a[3])};
                T2 = pk2(a[4], a[5]); T3 = pk2(a[6], a[7]);
            } else if (ph == 4) {
                W0 = (u32)__builtin_amdgcn_update_dpp((int)W0, (int)T2, 0x108, 0xf, 0x2, false);
                W1 = (u32)__builtin_amdgcn_update_dpp((int)W1, (int)T3, 0x108, 0xf, 0x2, false);
                asm volatile("" ::: "memory");
                *(LAS u2v*)(yb + 512 + 1024 * be) = u2v{W0, W1};
            } else if (ph == 5) {
                asm volatile("" ::: "memory"); *done = (unsigned char)(be + 1);
            }
        };
        auto rd = [&](int be, int h) __attribute__((always_inline)) {
            if (h == 0) {
                if (be == 0) { const u2v f2 = *(const volatile LAS u2v*)flp; fl[be] = f2[0]; fh[be] = f2[1]; }
                else if (be < 7) fl[be] = *(const volatile LAS u32*)flp;
                asm volatile("" ::: "memory");
                ma[be] = *(const LAS u32*)(ym + 1024 * be); mb[be] = *(const LAS u32*)(ym + 2 * Y_CONV + 1024 * be);
            } else {
                ca[be] = *(const LAS u32*)(yc + 1024 * be); cb[be] = *(const LAS u32*)(yc + 2 * Y_CONV + 1024 * be);
                if (be == 0) G2[be] = *(const LAS u32*)gl;
            }
        };
        auto rdu = [&](int be) __attribute__((always_inline)) {
            uu[be] = *(const LAS u32*)(xr + 512 * (7 - be));
            if (be > 0) G2[be] = *(const LAS u32*)(gl + 1024 * be);
        };
        u32 sfl;
        auto grab = [&](int be) __attribute__((always_inline)) {
            asm volatile("" : "+v"(ma[be]), "+v"(mb[be]), "+v"(ca[be]), "+v"(cb[be]), "+v"(fl[be]));
            if (be == 0) { asm volatile("" : "+v"(G2[0])); fl[0] &= fh[0]; }
            sfl = (u32)__builtin_amdgcn_readfirstlane(fl[be]);
        };
        auto ready = [&](int be) __attribute__((always_inline)) {
            return ((sfl + (u32)(0x7f - be) * 0x01010101u) & 0x80808080u) == 0x80808080u;
        };
        auto coef = [&](int be, int h) __attribute__((always_inline)) {
            if (h == 0) { ga = bf2f(G2[be]); gb = bfhi(G2[be]); c1a = 1.f - ga; PIN(c1a); c1b = 1.f - gb; PIN(c1b); }
            else { c1a *= bfhi(uu[be]); PIN(c1a); c1b *= bf2f(uu[be]); PIN(c1b); }
        };
        auto mix = [&](int be, int ph) __attribute__((always_inline)) {
            if (ph == -2) { grab(be); if (be > 0 && be < 7) coef(be, 0); }
            else if (ph == -1) {
                if (__builtin_expect(!ready(be), 0)) {
#pragma nounroll
                    for (u32 spins = 0; spins < SPIN_CAP; ++spins) { rd(be, 0); rd(be, 1); grab(be); if (ready(be)) break; }
                }
            } else if (ph == 0) { if (be == 0) coef(be, 0); }
            else if (ph == 1) { if (be < 7) coef(be, 1);
            } else if (ph == 2) {
                if (be == 0) { if (!(w & 1)) { ca[be] = 0u; cb[be] = 0u; } }
                x[0] = bf2f(mb[be]); x[1] = bfhi(mb[be]); x[2] = bf2f(cb[be]); x[3] = bfhi(cb[be]); x[4] = bf2f(ma[be]);
            } else if (ph == 3) {
                x[0] += cmask * x[2]; PIN(x[0]); x[1] += cmask * x[3]; PIN(x[1]);
                x[5] = bfhi(ma[be]); x[6] = bf2f(ca[be]); x[7] = bfhi(ca[be]);
            } else if (ph == 4) {
                x[4] += cmask * x[6]; PIN(x[4]); x[5] += cmask * x[7]; PIN(x[5]);
                x[0] *= c1a; PIN(x[0]); x[1] *= c1b; PIN(x[1]);
            } else if (ph == 5) {
                x[0] += ga * x[4]; PIN(x[0]); x[1] += gb * x[5]; PIN(x[1]);
                P = pk2(x[0], x[1]);
            } else if (ph == 6) {
                const u32 Q = (u32)__builtin_amdgcn_update_dpp(0, (int)P, 0xB1, 0xf, 0xf, true);
                __builtin_amdgcn_raw_buffer_store_b32(__builtin_amdgcn_perm(P, Q, sel), ors, oo, be * (256 * DM * 2), 0);
            }
        };
#pragma unroll
        for (int q = -3; q <= QRES; ++q) Br[q + 3] = ldB(q);
#pragma unroll
        for (int q = -3; q <= QRES; ++q) asm volatile("" : "+v"(Br[q + 3]));
        asm volatile("" : "+v"(yb), "+v"(ym), "+v"(yc), "+v"(xr), "+v"(bb), "+v"(gl) : "v"(oo), "v"(sel), "v"(cmask) : "memory");
        STAMP(7);
        __builtin_amdgcn_s_barrier();
        asm volatile("" ::: "memory");
        STAMP(8);
#pragma unroll
        for (int i = 0; i < 4; ++i) { ldA(i, 0); ldA(i, 1); }
        sfor<0, 8>([&](auto BE) __attribute__((always_inline)) {
            constexpr int be = BE.v;
            constexpr int ns = 4 * be > QRES ? 4 * be - QRES : 0;
            sfor<0, 4 * be + 4>([&](auto S) __attribute__((always_inline)) {
                constexpr int st = S.v;
                constexpr int i = st < 4 * be - ns ? st + ns : st < 4 * be ? st - (4 * be - ns) : st;
                constexpr int q = 4 * be - i;
                const u4v bfr = (q > QRES) ? Bs[i & 7] : Br[q > QRES ? 0 : q + 3];
                if (st == 0) acc[be & 1] = __builtin_amdgcn_mfma_f32_32x32x16_bf16(__builtin_bit_cast(bf8, A[i]), __builtin_bit_cast(bf8, bfr), f16v{}, 0, 0, 0);
                else acc[be & 1] = __builtin_amdgcn_mfma_f32_32x32x16_bf16(__builtin_bit_cast(bf8, A[i]), __builtin_bit_cast(bf8, bfr), acc[be & 1], 0, 0, 0);
                __builtin_amdgcn_sched_barrier(0);
                if (be == 0) { ldA(4 + st, 0); ldA(4 + st, 1); }
                if (be == 1 && st == B2STEP) { asm volatile("" ::: "memory"); __builtin_amdgcn_s_barrier(); asm volatile("" ::: "memory"); }
                if (be == 1 && st >= 6) { ldA(8 + st - 6, 0); ldA(8 + st - 6, 1); }
                if (be == 2 && st < 2) { ldA(10 + st, 0); ldA(10 + st, 1); }
                if (be == 2 && st >= 8) { ldA(12 + st - 8, 0); ldA(12 + st - 8, 1); }
                if (be == 3 && st >= 8) ldA(16 + ((st - 8) >> 1), st & 1);
                if (be == 4 && st >= 12) ldA(20 + ((st - 12) >> 1), st & 1);
                if (be == 6 && st >= 2 && st < 10) ldA(24 + ((st - 2) >> 1), st & 1);
                if (be == 7 && st >= 2 && st < 10) ldA(28 + ((st - 2) >> 1), st & 1);
                {
                    constexpr int i2 = st + PD - (4 * be - ns);
                    if (i2 >= 0 && i2 < ns) Bs[i2 & 7] = ldB(4 * be - i2);
                }
                if (be > 1 && st < 2) {
                    __builtin_amdgcn_s_setprio(0); __builtin_amdgcn_s_setprio(0); __builtin_amdgcn_s_setprio(0);
                    if (be != 3 && be != 4 && be != 5 && be != 6) { __builtin_amdgcn_s_setprio(0); __builtin_amdgcn_s_setprio(0); }
                }
                if (be == 1 && st < 2) { __builtin_amdgcn_s_setprio(0); __builtin_amdgcn_s_setprio(0); }
                if (be > 0 && st >= 2) post(be - 1, st - 2);
                constexpr int rb[7] = {3, 4, 5, 5, 6, 6, 7}, rs[7] = {0, 0, 0, 8, 0, 14, 14};
                constexpr int cbt[7] = {3, 4, 5, 5, 6, 6, 7}, cs[7] = {7, 8, 8, 16, 10, 20, 20};
#pragma unroll
                for (int m = 0; m < 7; ++m) {
                    if (be == rb[m] && st == rs[m]) rd(m, 0);
                    if (be == rb[m] && st == rs[m] + 1) rd(m, 1);
                    if (be == rb[m] && st == rs[m] + 2) rdu(m);
                }
                sfor<0, 7>([&](auto M) __attribute__((always_inline)) {
                    constexpr int m = M.v;
                    constexpr int r = st - cs[m];
                    if constexpr (be == cbt[m] && r >= 0 && r < 2) mix(m, r - 2);
                    if constexpr (be == cbt[m] && m == 0 && r >= 2 && r < 9) mix(m, r - 2);
                    if constexpr (be == cbt[m] && m > 0 && r >= 2 && r < 8) mix(m, r - 1);
                });
                if (be == 7 && st == 22) rdu(7);
                if (be == 7 && st == 28) coef(7, 0);
                if (be == 7 && st == 29) coef(7, 1);
                __builtin_amdgcn_sched_barrier(0);
            });
            if (be == 0) STAMP(9);
            if (be == 3) STAMP(10);
        });
        STAMP(11);
#pragma unroll
        for (int ph = 0; ph < 5; ++ph) post(7, ph);
        asm volatile("s_waitcnt lgkmcnt(0)" ::: "memory");
        __builtin_amdgcn_s_barrier();
        asm volatile("" ::: "memory");
        rd(7, 0); rd(7, 1);
        sfor<2, 7>([&](auto PH) __attribute__((always_inline)) { mix(7, PH.v); });
        STAMP(12);
    }

    STAMP_DUMP;
}

extern "C" void kernel_launch(void* const* d_in, const int* in_sizes, int n_in,
                              void* d_out, int out_size, void* d_ws, size_t ws_size,
                              hipStream_t stream) {
    fused_k<<<256, 512, 0, stream>>>((const u16*)d_in[0], (const u16*)d_in[1], (const u16*)d_in[2], (const u16*)d_in[3], (const u16*)d_in[4],
             (const u16*)d_in[5], (const u16*)d_in[6], (const u16*)d_in[8], (const u16*)d_in[9], (const u16*)d_in[10],
             (const u16*)d_in[11], (const u16*)d_in[12], (const u16*)d_in[13], (u16*)d_out, (char*)d_ws);
}
```

```cpp
#define GRP 0
#define SIDX 1
#define STATMAX 0

#include <hip/hip_runtime.h>
#define NSTAMP 14
#define CODE_BYTES 15872
#define STAMP_DECL unsigned long long st_[NSTAMP] = {}
#define STAMP(i) do { if ((i) == 0 || (i) == SIDX) { __builtin_amdgcn_sched_barrier(0); asm volatile("s_memrealtime %0" : "+s"(st_[i])); __builtin_amdgcn_sched_barrier(0); } } while (0)
#define STAMP_DUMP do { asm volatile("s_waitcnt vmcnt(0)" ::: "memory"); STAMP(13); asm volatile("s_waitcnt lgkmcnt(0)" ::: "memory"); \
    if ((threadIdx.x & 63) == 0) { unsigned long long* p_ = (unsigned long long*)(ws + (16u << 20)) + (blockIdx.x * 8 + (threadIdx.x >> 6)) * 16; \
        p_[0] = st_[0]; p_[1] = st_[SIDX]; } } while (0)
#define kernel_launch kernel_launch_inner
#include <hip/hip_runtime.h>
#include <stdint.h>

typedef unsigned short u16;
typedef unsigned int u32;
typedef unsigned long long u64;
typedef __attribute__((ext_vector_type(2))) __bf16 bf2;
typedef __attribute__((ext_vector_type(8))) __bf16 bf8;
typedef __attribute__((ext_vector_type(4))) short s4v;
typedef __attribute__((ext_vector_type(2))) u32 u2v;
typedef __attribute__((ext_vector_type(3))) u32 u3v;
typedef __attribute__((ext_vector_type(4))) u32 u4v;
typedef __attribute__((ext_vector_type(2))) float f2v;
typedef __attribute__((ext_vector_type(4))) float f4v;
typedef __attribute__((ext_vector_type(16))) float f16v;
#define GAS __attribute__((address_space(1)))
#define LAS __attribute__((address_space(3)))
typedef GAS u64 gu64;
#define RLX_AGENT __ATOMIC_RELAXED, __HIP_MEMORY_SCOPE_AGENT

#define NT " nt"
#define POLL_SLEEP0 4
#define POLL_SLEEP 2
#define CODE_PF 8
#define BSPLIT 1
#define HPRIO 0
#define GSCOPE __HIP_MEMORY_SCOPE_AGENT
#define QRES 13
#define RS0 3
#define GEARLY 1
#define POLL_ATOMIC 1
#define GPOS 0
#define TWOSTEP 1
#define B2STEP 5
#define WPRE 0
#define GSLEEP 8
#define P1PRIO 1
#define WBL2 0
#define TOUCH 1

constexpr int SEQ = 2048, DM = 512, NS = 128;

constexpr int XC_OFF = 0, XC_CONV = 8384, XC_COPY = 4160;
constexpr int Z_OFF = 33536, Z_CONV = 4608, Z_PAD = 512;
constexpr int Y_OFF = 51968, Y_CONV = 8256;
constexpr int TAB_OFF = 84992, TAB_CH = 32768;
constexpr int FL_OFF = 150528;
constexpr int LDS_BYTES = 150912;
constexpr int TU_OFF = Y_OFF, TV_OFF = Y_OFF + 8192, TK_OFF = Y_OFF + 16384;
constexpr int WS_FLAG = 16384, WS_G = 65536, WS_X = 1 << 20;
constexpr u32 SPIN_CAP = 1u << 17;

__device__ __forceinline__ float bf2f(u32 x) { return __uint_as_float(x << 16); }
__device__ __forceinline__ float bfhi(u32 x) { return __uint_as_float(x & 0xffff0000u); }
__device__ __forceinline__ u32 pk2(float a, float b) { bf2 r; r[0] = (__bf16)a; r[1] = (__bf16)b; return __builtin_bit_cast(u32, r); }
__device__ __forceinline__ float rbf(float a) { return __uint_as_float(pk2(0.f, a)); }
__device__ __forceinline__ float dot2(u32 a, u32 b, float c) {
    return __builtin_amdgcn_fdot2_f32_bf16(__builtin_bit_cast(bf2, a), __builtin_bit_cast(bf2, b), c, false);
}
struct cmult { f2v b, bs; };
__device__ __forceinline__ cmult mkm(f2v b) { return cmult{b, f2v{-b[1], b[0]}}; }
__device__ __forceinline__ f2v cmul(f2v a, const cmult& m) {
    return __builtin_elementwise_fma(f2v{a[0], a[0]}, m.b, f2v{a[1], a[1]} * m.bs);
}
__device__ __forceinline__ f2v cexp_p(float lz, float rev, float p) {
    const float m = __builtin_amdgcn_exp2f(lz * p);
    const float a = __builtin_amdgcn_fractf(rev * p);
    return f2v{m * __builtin_amdgcn_cosf(a), m * __builtin_amdgcn_sinf(a)};
}
template <int CTRL> __device__ __forceinline__ float dppz(float v) {
    return __int_as_float(__builtin_amdgcn_update_dpp(0, __float_as_int(v), CTRL, 0xf, 0xf, true));
}
template <int I> struct ic { static constexpr int v = I; };
template <int B, int E, class F> __device__ __forceinline__ void sfor(F&& f) {
    if constexpr (B < E) { f(ic<B>{}); sfor<B + 1, E>(f); }
}
__device__ __forceinline__ void dma16(const void* base, u32 off, u32 lds) {
    u32 keep;
    asm volatile("s_mov_b32 %0, m0\n\ts_mov_b32 m0, %3\n\ts_nop 0\n\tglobal_load_lds_dwordx4 %1, %2" NT "\n\ts_mov_b32 m0, %0"
                 : "=&s"(keep) : "v"(off), "s"(base), "s"(lds) : "memory");
}

__global__ void __launch_bounds__(512, 2)
fused_k(const u16* __restrict__ gu, const u16* __restrict__ glar, const u16* __restrict__ gaim,
        const u16* __restrict__ gcre, const u16* __restrict__ gcim, const u16* __restrict__ gldt,
        const u16* __restrict__ gdsk, const u16* __restrict__ gk, const u16* __restrict__ gv,
        const u16* __restrict__ gshk, const u16* __restrict__ gssk, const u16* __restrict__ gssd,
        const u16* __restrict__ ggw, u16* __restrict__ gout, char* ws)
{
    __shared__ __attribute__((aligned(16))) char smem[LDS_BYTES];
    LAS char* sm = (LAS char*)smem;
    const u32 smb = (u32)(size_t)sm;
    const int tid = threadIdx.x, l = tid & 63;
    const int w = __builtin_amdgcn_readfirstlane(tid >> 6);
    const int bx = blockIdx.x & 7, bj = blockIdx.x >> 3;
    const int d0 = 64 * bx + 2 * bj;
    STAMP_DECL;
    STAMP(0);
    if (w == 7) {
        u64 pc; u32 keep;
        asm volatile("s_getpc_b64 %0\n\ts_mov_b32 %1, m0\n\ts_mov_b32 m0, %3\n\ts_nop 0\n\tglobal_load_lds_dword %2, %0\n\ts_mov_b32 m0, %1"
                     : "=&s"(pc), "=&s"(keep) : "v"(min((u32)((bj * CODE_PF + (l & (CODE_PF - 1))) & 127) * 128u, (u32)CODE_BYTES)), "s"(smb + FL_OFF + 128) : "memory");
    }
    u32 go, po, so_;
    {
        const int r = 8 * w + (l >> 3);
        const int f = ((r >> 1) & 1) | (((r >> 3) & 1) << 1);
        go = (u32)(64 * bj + r) * (DM * 2) + 128 * bx + 16 * ((l & 7) ^ (f << 1));
        po = 2u * (u32)((d0 + (w & 1)) * NS + 64 * ((w >> 1) & 1) + l);
        so_ = 8u * (u32)(64 * bx + 16 * (w & 3) + (l & 15));
        asm volatile("" : "+v"(go), "+v"(po), "+v"(so_));
    }
    asm volatile("" :: "s"(gu), "s"(glar), "s"(gaim), "s"(gcre), "s"(gcim), "s"(gldt), "s"(gdsk), "s"(gk), "s"(gv), "s"(gshk), "s"(gssk), "s"(gssd), "s"(ggw), "s"(gout), "s"(ws));
    const __amdgpu_buffer_rsrc_t xrs = __builtin_amdgcn_make_buffer_rsrc(ws + WS_X, 0, DM * 2 * 4096, 0x00020000);

    u32 p_lar, p_aim, p_cre, p_cim;
    if (w < 4) {
        asm volatile("global_load_ushort %0, %4, %5" NT "\n\tglobal_load_ushort %1, %4, %6" NT "\n\t"
                     "global_load_ushort %2, %4, %7" NT "\n\tglobal_load_ushort %3, %4, %8" NT
                     : "=&v"(p_lar), "=&v"(p_aim), "=&v"(p_cre), "=&v"(p_cim)
                     : "v"(po), "s"(glar), "s"(gaim), "s"(gcre), "s"(gcim) : "memory");
    } else if (w == 5 && bj > 0) {
        dma16(gk, go - 48 * DM * 2, smb + TK_OFF);
    }
    dma16(gk, go, smb + TK_OFF + 1024 + 1024 * w);
    dma16(gv, go, smb + TV_OFF + 1024 * w);
    dma16(gu, go, smb + TU_OFF + 1024 * w);
    auto late = [&](u2v& shk, u64& tag) __attribute__((always_inline)) {
        asm volatile("global_load_dwordx2 %0, %2, %3\n\ts_load_dwordx2 %1, %4, 0x0" : "=&v"(shk), "=&s"(tag) : "v"(so_), "s"(gshk), "s"(ws) : "memory");
    };

    const int cg = w & 3, th = w >> 2;
    auto phase1 = [&](u2v& shk, u64& tag, bool mwave) __attribute__((always_inline)) {
        const int G = l >> 4, q = (l >> 2) & 3, p = l & 3;
        const int row = 32 * th + 8 * G + 7 - q;
        const int f = ((row >> 1) & 1) | (((row >> 3) & 1) << 1);
        u32 a0 = smb + TU_OFF - 512 + row * 128 + ((cg ^ f) << 5) + 8 * p;
        u32 a2 = smb + TK_OFF + row * 128 + ((cg ^ f ^ 2) << 5) + 8 * p;
        u32 xo = (u32)(bx * 32 + bj) * 16384u + (u32)((8 * cg + ((l & 15) >> 1)) * 512 + th * 256 + (l & 1) * 64 + G * 16);
        float ev[8] = {0.f, 0.f, 0.f, 0.f, 0.f, 0.f, 0.f, 0.f};
        asm volatile("" : "+v"(a0), "+v"(a2), "+v"(xo), "+v"(ev[0]), "+v"(ev[1]), "+v"(ev[2]), "+v"(ev[3]), "+v"(ev[4]), "+v"(ev[5]), "+v"(ev[6]), "+v"(ev[7]));
        STAMP(2);
        if (mwave) asm volatile("s_waitcnt vmcnt(0) lgkmcnt(0)" : "+v"(shk), "+s"(tag) :: "memory");
        else asm volatile("s_waitcnt vmcnt(3) lgkmcnt(0)" : "+v"(shk), "+s"(tag) :: "memory");
        tag += 1ull;
        if (P1PRIO) __builtin_amdgcn_s_setprio(3);
        __builtin_amdgcn_s_barrier();
        STAMP(3);
        typedef LAS s4v* trp;
        auto tr = [&](u32 a) __attribute__((always_inline)) { return __builtin_bit_cast(u2v, __builtin_amdgcn_ds_read_tr16_b64_v4i16((trp)(size_t)a)); };
        const u2v ka = tr(a0 + (TK_OFF - TU_OFF) + 1024 + 512), kb = tr(a0 + (TK_OFF - TU_OFF) + 1024), kc = tr(a2);
        const u2v ua = tr(a0 + 512), ub = tr(a0);
        const u2v va = tr(a0 + (TV_OFF - TU_OFF) + 512), vb = tr(a0 + (TV_OFF - TU_OFF));
        __builtin_amdgcn_sched_barrier(0);
        const u32 K[6] = {ka[0], ka[1], kb[0], kb[1], kc[0], kc[1]};
        const u32 V[4] = {va[0], va[1], vb[0], vb[1]};
        u32 Ks[5];
#pragma unroll
        for (int n = 0; n < 5; ++n) Ks[n] = __builtin_amdgcn_alignbit(K[n + 1], K[n], 16);
#pragma unroll
        for (int n = 0; n < 4; ++n) { ev[2 * n] = dot2(K[n], shk[0], ev[2 * n]); ev[2 * n + 1] = dot2(Ks[n], shk[0], ev[2 * n + 1]); }
        u4v ud{ua[0], ua[1], ub[0], ub[1]};
        __builtin_amdgcn_raw_buffer_store_b128(ud, xrs, xo, 0, 16);
        __builtin_amdgcn_sched_barrier(0);
#pragma unroll
        for (int n = 0; n < 4; ++n) { ev[2 * n] = dot2(K[n + 1], shk[1], ev[2 * n]); ev[2 * n + 1] = dot2(Ks[n + 1], shk[1], ev[2 * n + 1]); }
        u4v kv;
#pragma unroll
        for (int n = 0; n < 4; ++n) kv[n] = pk2(ev[2 * n] * bf2f(V[n]), ev[2 * n + 1] * bfhi(V[n]));
        __builtin_amdgcn_raw_buffer_store_b128(kv, xrs, xo + 128u, 0, 16);
        asm volatile("" :: "v"(ud));
        __builtin_amdgcn_sched_barrier(0);
        if (P1PRIO) __builtin_amdgcn_s_setprio(0);
        STAMP(4);
    };
    auto signal = [&](u64 tag) __attribute__((always_inline)) {
        if (l == 0) __hip_atomic_store((gu64*)(ws + WS_FLAG + ((bx * 4 + cg) * 4 + ((bj >> 1) & 3)) * 128) + ((bj >> 3) * 2 + (bj & 1)) * 2 + th, tag, RLX_AGENT);
        STAMP(5);
    };

    if (__builtin_expect(w >= 4, 0)) {
    const int cw = w - 4;
    if (HPRIO) __builtin_amdgcn_s_setprio(HPRIO);
    u64 tag; u2v shk;
    {
        const u4v z4{0, 0, 0, 0};
        const int zo = (l < 32) ? Z_OFF + cw * Z_CONV + 16 * l
                                : (l < 35) ? XC_OFF + cw * XC_CONV + 4096 + (l == 32 ? 0 : XC_COPY - 16 * (l - 33)) : FL_OFF + 16 * ((l - 35) & 7);
        *(LAS u4v*)(sm + zo) = z4;
        *(LAS u4v*)(sm + ((w == 5 && bj == 0) ? TK_OFF + 16 * l : zo)) = z4;
    }
    late(shk, tag);
    if (GSLEEP) __builtin_amdgcn_s_sleep(GSLEEP);
    u4v gr0, gr1, gwv;
    {
        const u32 ro = (u32)(8 * blockIdx.x + 2 * cw) * (DM * 2) + 16 * l;
        asm volatile("global_load_dwordx4 %0, %3, %5" NT "\n\tglobal_load_dwordx4 %1, %3, %5 offset:1024" NT "\n\tglobal_load_dwordx4 %2, %4, %6"
                     : "=&v"(gr0), "=&v"(gr1), "=&v"(gwv) : "v"(ro), "v"(16u * l), "s"(gu), "s"(ggw) : "memory");
    }
    STAMP(1);
    phase1(shk, tag, false);
    {
        asm volatile("s_waitcnt vmcnt(2)" : "+v"(gr0), "+v"(gr1), "+v"(gwv) :: "memory");
        const float s0 = dot2(gr0[0], gwv[0], dot2(gr0[1], gwv[1], dot2(gr0[2], gwv[2], dot2(gr0[3], gwv[3], 0.f))));
        const float s1 = dot2(gr1[0], gwv[0], dot2(gr1[1], gwv[1], dot2(gr1[2], gwv[2], dot2(gr1[3], gwv[3], 0.f))));
        float ee = ((l & 1) ? s1 : s0) + dppz<0xB1>((l & 1) ? s0 : s1);
        ee += dppz<0x4E>(ee); ee += dppz<0x124>(ee); ee += dppz<0x128>(ee);
        const float r0 = __int_as_float(__builtin_amdgcn_readlane(__float_as_int(ee), 0)) + __int_as_float(__builtin_amdgcn_readlane(__float_as_int(ee), 16))
                       + __int_as_float(__builtin_amdgcn_readlane(__float_as_int(ee), 32)) + __int_as_float(__builtin_amdgcn_readlane(__float_as_int(ee), 48));
        const float r1 = __int_as_float(__builtin_amdgcn_readlane(__float_as_int(ee), 1)) + __int_as_float(__builtin_amdgcn_readlane(__float_as_int(ee), 17))
                       + __int_as_float(__builtin_amdgcn_readlane(__float_as_int(ee), 33)) + __int_as_float(__builtin_amdgcn_readlane(__float_as_int(ee), 49));
        const float g0 = __builtin_amdgcn_rcpf(1.f + __builtin_amdgcn_exp2f(-1.44269504088896341f * rbf(r0)));
        const float g1 = __builtin_amdgcn_rcpf(1.f + __builtin_amdgcn_exp2f(-1.44269504088896341f * rbf(r1)));
        const int row = 8 * blockIdx.x + 2 * cw;
        const int be = row >> 8, cc = (row >> 3) & 31, hh = (row >> 2) & 1;
        const int gi = (((cc >> 4) + 2 * hh) * 8 + be) * 32 + (cc & 15) * 2 + (cw & 1);
        if (l == 0) __hip_atomic_store((gu64*)(ws + WS_G) + gi, (tag << 32) | pk2(g0, g1), RLX_AGENT);
        asm volatile("s_waitcnt vmcnt(1)" ::: "memory");
        signal(tag);
    }
    gu64* gp = (gu64*)(ws + WS_G) + cw * 256 + (l >> 1);
    u64 gg[8];
    {
        gu64* fp = (gu64*)(ws + WS_FLAG + ((bx * 4 + (bj >> 3)) * 4 + cw) * 128) + (l & 15);
        if (POLL_SLEEP0) __builtin_amdgcn_s_sleep(POLL_SLEEP0);
        if (l < 16) {
            const u64 zero = 0;
#pragma nounroll
            for (u32 spins = 0; spins < SPIN_CAP; ++spins) {
                u64 v;
                asm volatile("global_atomic_add_x2 %0, %1, %2, off sc0 sc1\n\ts_waitcnt vmcnt(0)" : "=&v"(v) : "v"(fp), "v"(zero) : "memory");
                if (__all(v == tag)) break;
                __builtin_amdgcn_s_sleep(POLL_SLEEP);
            }
        }
        asm volatile("" ::: "memory");
        STAMP(6);
        const u32 xo = (u32)(bx * 32 + 2 * cw + (l >> 5)) * 16384u + (u32)(bj * 512 + (l & 31) * 16);
        u4v xv[4];
#pragma unroll
        for (int pc = 0; pc < 4; ++pc) xv[pc] = __builtin_amdgcn_raw_buffer_load_b128(xrs, xo + 131072u * pc, 0, 16);
#pragma unroll
        for (int be = 0; be < 8; ++be) gg[be] = __hip_atomic_load(gp + 32 * be, RLX_AGENT);
        LAS char* xb = sm + XC_OFF + ((l >> 2) & 3) * XC_CONV + 2 * (2040 - 64 * (2 * cw + (l >> 5)) - 32 * ((l >> 4) & 1) - 8 * (l & 3));
#pragma unroll
        for (int pc = 0; pc < 4; ++pc) {
            LAS char* p = xb - 1024 * pc;
            *(LAS u4v*)p = xv[pc];
            *(LAS u3v*)(p + XC_COPY) = u3v{__builtin_amdgcn_alignbit(xv[pc][1], xv[pc][0], 16), __builtin_amdgcn_alignbit(xv[pc][2], xv[pc][1], 16),
                                           __builtin_amdgcn_alignbit(xv[pc][3], xv[pc][2], 16)};
            *(LAS u16*)(p + XC_COPY + 12) = (u16)(xv[pc][3] >> 16);
            *(LAS u16*)(p + XC_COPY - 2) = (u16)xv[pc][0];
            if (pc == 0) {
                asm volatile("s_waitcnt lgkmcnt(0)" ::: "memory");
                __builtin_amdgcn_s_barrier();
                asm volatile("" ::: "memory");
            }
        }
    }
    STAMP(7);
    asm volatile("s_waitcnt lgkmcnt(0)" ::: "memory");
    __builtin_amdgcn_s_barrier();
    STAMP(8);

    {
#pragma nounroll
        for (u32 spins = 0; spins < SPIN_CAP; ++spins) {
            u32 bad = 0;
#pragma unroll
            for (int be = 0; be < 8; ++be) bad |= (u32)(gg[be] >> 32) ^ (u32)tag;
            if (__all(bad == 0)) break;
            __builtin_amdgcn_s_sleep(4);
#pragma unroll
            for (int be = 0; be < 8; ++be) gg[be] = __hip_atomic_load(gp + 32 * be, RLX_AGENT);
        }
        STAMP(9);
#pragma unroll
        for (int be = 0; be < 8; ++be) *(LAS u32*)(sm + TAB_OFF + 1024 * be + 256 * cw + 4 * l) = (u32)gg[be];
        asm volatile("" ::: "memory");
        if (l == 0) *(volatile LAS unsigned char*)(sm + FL_OFF + 4 + cw) = 0xff;
        if (blockIdx.x == 0) {
            if (l == 0) {
                const u32 old = __hip_atomic_fetch_add((LAS u32*)(sm + FL_OFF + 64), 1u, __ATOMIC_RELAXED, __HIP_MEMORY_SCOPE_WORKGROUP);
                if (old == 3u) __hip_atomic_store((gu64*)ws, tag, RLX_AGENT);
            }
        }
        STAMP(10);
        if (WBL2 && bj == 0 && cw == 0) {
#pragma nounroll
            for (u32 spins = 0; spins < SPIN_CAP; ++spins) { if ((*(volatile LAS u32*)(sm + FL_OFF) & 0xffu) >= WBL2) break; __builtin_amdgcn_s_sleep(2); }
            asm volatile("buffer_wbl2 sc1" ::: "memory");
        }
    }
    STAMP_DUMP;
    return;
    }

    {
        u64 tag; u2v shk; u32 p_ldt, skip;
        asm volatile("s_load_dword %0, %2, %4\n\ts_load_dword %1, %3, %4"
                     : "=&s"(p_ldt), "=&s"(skip) : "s"(gldt), "s"((w & 2) ? gssd : gdsk), "s"(2 * d0) : "memory");
        late(shk, tag);
        asm volatile("s_waitcnt vmcnt(4) lgkmcnt(0)" : "+v"(p_lar), "+v"(p_aim), "+v"(p_cre), "+v"(p_cim), "+s"(p_ldt), "+s"(skip) :: "memory");
        p_ldt = (p_ldt >> (16 * (w & 1))) & 0xffffu;
        skip = (skip >> (16 * (w & 1))) & 0xffffu;
        STAMP(1);
        u4v ta_[8], tb_[8];
        const float dt = rbf(expf(bf2f(p_ldt)));
        const float zr = -rbf(expf(bf2f(p_lar))) * dt;
        const float zi = bf2f(p_aim) * dt;
        const float lz = zr * 1.44269504088896341f, rev = zi * 0.15915494309189535f;
        const f2v e8 = cexp_p(lz, rev, 8.f);
        const cmult E8 = mkm(e8);
        {
            const cmult E1 = mkm(cexp_p(lz, rev, 1.f)), E4 = mkm(cexp_p(lz, rev, 4.f)), E256 = mkm(cexp_p(lz, rev, 256.f));
            f2v a0[16], a1[16];
            a0[0] = f2v{2.f * (bf2f(p_cre) * dt), 2.f * (bf2f(p_cim) * dt)};
            a0[4] = cmul(a0[0], E4);
#pragma unroll
            for (int g = 1; g < 4; ++g) { a0[g] = cmul(a0[g - 1], E1); a0[g + 4] = cmul(a0[g + 3], E1); }
            a0[8] = cmul(a0[0], E256); a0[9] = cmul(a0[2], E256); a0[10] = cmul(a0[4], E256); a0[11] = cmul(a0[6], E256);
            a0[12] = cmul(a0[8], E8);
            a0[13] = cmul(a0[8], E256); a0[14] = cmul(a0[10], E256); a0[15] = cmul(a0[12], E256);
            a1[0] = cmul(a0[13], E256); a1[1] = cmul(a0[14], E256); a1[2] = cmul(a0[15], E256);
            a1[3] = cmul(a1[0], E256); a1[4] = cmul(a1[2], E256);
            a1[5] = cmul(a1[3], E256); a1[6] = cmul(a1[4], E256);
            a1[7] = cmul(a1[5], E256); a1[8] = cmul(a1[6], E256);
            a1[9] = cmul(a1[7], E256); a1[10] = cmul(a1[8], E256);
#pragma unroll
            for (int g = 11; g < 16; ++g) a1[g] = f2v{0.f, 0.f};
#pragma unroll
            for (int cc = 0; cc < 4; ++cc) {
                const int g = 4 * cc;
                ta_[2 * cc] = u4v{pk2(a0[g][0], a0[g + 1][0]), pk2(a0[g + 2][0], a0[g + 3][0]), pk2(a1[g][0], a1[g + 1][0]), pk2(a1[g + 2][0], a1[g + 3][0])};
                ta_[2 * cc + 1] = u4v{pk2(-a0[g][1], -a0[g + 1][1]), pk2(-a0[g + 2][1], -a0[g + 3][1]), pk2(-a1[g][1], -a1[g + 1][1]), pk2(-a1[g + 2][1], -a1[g + 3][1])};
                asm volatile("" : "+v"(ta_[2 * cc]), "+v"(ta_[2 * cc + 1]));
            }
        }
        const cmult E64 = mkm(cexp_p(lz, rev, 64.f));
        f2v e[8];
        e[0] = f2v{1.f, 0.f}; e[1] = e8;
        e[4] = cexp_p(lz, rev, 32.f);
        e[2] = cmul(e[1], E8); e[5] = cmul(e[4], E8); e[3] = cmul(e[2], E8);
        e[6] = cmul(e[5], E8); e[7] = cmul(e[6], E8);
        auto btab = [&](int cc) __attribute__((always_inline)) {
            if (cc > 0) {
#pragma unroll
                for (int g = 0; g < 8; ++g) e[g] = cmul(e[g], E64);
            }
            tb_[2 * cc] = u4v{pk2(e[0][0], e[1][0]), pk2(e[2][0], e[3][0]), pk2(e[4][0], e[5][0]), pk2(e[6][0], e[7][0])};
            tb_[2 * cc + 1] = u4v{pk2(e[0][1], e[1][1]), pk2(e[2][1], e[3][1]), pk2(e[4][1], e[5][1]), pk2(e[6][1], e[7][1])};
            asm volatile("" : "+v"(tb_[2 * cc]), "+v"(tb_[2 * cc + 1]));
        };
#pragma unroll
        for (int cc = 0; cc < BSPLIT; ++cc) btab(cc);
        phase1(shk, tag, true);
        LAS char* tab = sm + TAB_OFF + w * 16384;
        {
            LAS char* tw = tab + l * 64;
            const int sw = (l >> 1) & 3;
#pragma unroll
            for (int cc = BSPLIT; cc < 4; ++cc) btab(cc);
#pragma unroll
            for (int cc = 0; cc < WPRE; ++cc) { *(LAS u4v*)(tw + ((cc ^ sw) << 4)) = ta_[2 * cc]; *(LAS u4v*)(tw + 4096 + ((cc ^ sw) << 4)) = ta_[2 * cc + 1]; }
            asm volatile("s_waitcnt vmcnt(0)" ::: "memory");
            signal(tag);
            if (w >= 2) {
                const u32 so = (u32)(d0 + (w & 1)) * (SEQ * 2) + 16 * l;
                const u32 zd = smb + Z_OFF + w * Z_CONV + Z_PAD;
#pragma unroll
                for (int pc = 0; pc < 4; ++pc) dma16(gssk, so + 1024 * pc, zd + 1024 * pc);
            }
#pragma unroll
            for (int cc = WPRE; cc < 4; ++cc) { *(LAS u4v*)(tw + ((cc ^ sw) << 4)) = ta_[2 * cc]; *(LAS u4v*)(tw + 4096 + ((cc ^ sw) << 4)) = ta_[2 * cc + 1]; }
#pragma unroll
            for (int cc = 0; cc < 4; ++cc) { *(LAS u4v*)(tw + 8192 + ((cc ^ sw) << 4)) = tb_[2 * cc]; *(LAS u4v*)(tw + 12288 + ((cc ^ sw) << 4)) = tb_[2 * cc + 1]; }
        }
        {
            f16v kd = {};
            const int h = l >> 5, blk = (l >> 4) & 1, q = (l >> 2) & 3, p = l & 3;
            typedef LAS s4v* trp;
            const int b0 = (8 * h + q) * 64 + (((2 * blk + (p >> 1)) ^ (q >> 1)) << 4) + 8 * (p & 1);
            const int b1 = (8 * h + 4 + q) * 64 + (((2 * blk + (p >> 1)) ^ (2 + (q >> 1))) << 4) + 8 * (p & 1);
            u4v fa[8], fb[8];
#pragma unroll
            for (int s = 0; s < 8; ++s) {
                u2v x0 = __builtin_bit_cast(u2v, __builtin_amdgcn_ds_read_tr16_b64_v4i16((trp)(tab + s * 1024 + b0)));
                u2v x1 = __builtin_bit_cast(u2v, __builtin_amdgcn_ds_read_tr16_b64_v4i16((trp)(tab + s * 1024 + b1)));
                u2v y0 = __builtin_bit_cast(u2v, __builtin_amdgcn_ds_read_tr16_b64_v4i16((trp)(tab + 8192 + s * 1024 + b0)));
                u2v y1 = __builtin_bit_cast(u2v, __builtin_amdgcn_ds_read_tr16_b64_v4i16((trp)(tab + 8192 + s * 1024 + b1)));
                fa[s] = u4v{x0[0], x0[1], x1[0], x1[1]}; fb[s] = u4v{y0[0], y0[1], y1[0], y1[1]};
                if (s >= 2) kd = __builtin_amdgcn_mfma_f32_32x32x16_bf16(__builtin_bit_cast(bf8, fa[s - 2]), __builtin_bit_cast(bf8, fb[s - 2]), kd, 0, 0, 0);
                __builtin_amdgcn_sched_barrier(0);
            }
#pragma unroll
            for (int s = 6; s < 8; ++s) kd = __builtin_amdgcn_mfma_f32_32x32x16_bf16(__builtin_bit_cast(bf8, fa[s]), __builtin_bit_cast(bf8, fb[s]), kd, 0, 0, 0);
            volatile LAS u32* pairf = (volatile LAS u32*)(sm + FL_OFF + 16 + 4 * (w & 1));
            if (w >= 2) {
#pragma unroll
                for (int g = 0; g < 4; ++g) *(LAS f4v*)(tab + g * 1024 + l * 16) = f4v{kd[4 * g], kd[4 * g + 1], kd[4 * g + 2], kd[4 * g + 3]};
                asm volatile("" ::: "memory");
                if (l == 0) *pairf = 1u;
                asm volatile("s_waitcnt vmcnt(0)" ::: "memory");
                LAS u16* z0 = (LAS u16*)(sm + Z_OFF + w * Z_CONV + Z_PAD);
                if (l == 0) *z0 = (u16)pk2(bf2f(*z0) + bf2f(skip), 0.f);
            } else {
#pragma nounroll
                for (u32 spins = 0; spins < SPIN_CAP; ++spins) if (*pairf != 0u) break;
                asm volatile("" ::: "memory");
#pragma unroll
                for (int g = 0; g < 4; ++g) {
                    const f4v o = *(LAS f4v*)(tab + 32768 + g * 1024 + l * 16);
                    kd[4 * g] += o[0]; kd[4 * g + 1] += o[1]; kd[4 * g + 2] += o[2]; kd[4 * g + 3] += o[3];
                }
                if (l == 0) kd[0] += bf2f(skip);
                LAS char* zb = sm + Z_OFF + w * Z_CONV + Z_PAD + 16 * (l & 31);
                if (l < 32) {
                    *(LAS u4v*)(zb) = u4v{pk2(kd[0], kd[1]), pk2(kd[2], kd[3]), pk2(kd[4], kd[5]), pk2(kd[6], kd[7])};
                    *(LAS u4v*)(zb + 512) = u4v{pk2(kd[8], kd[8]), pk2(kd[9], kd[10]), pk2(kd[10], kd[10]), pk2(kd[11], kd[12])};
                    *(LAS u4v*)(zb + 1024) = u4v{pk2(kd[13], kd[13]), pk2(kd[13], kd[14]), pk2(kd[14], kd[14]), pk2(kd[15], kd[15])};
                } else {
                    *(LAS u4v*)(zb + 1536) = u4v{pk2(kd[0], kd[0]), pk2(kd[0], kd[1]), pk2(kd[1], kd[1]), pk2(kd[2], kd[2])};
#pragma unroll
                    for (int a = 0; a < 4; ++a) {
                        const float lo = kd[3 + 2 * a], hi = kd[4 + 2 * a];
                        const u32 ll = pk2(lo, lo);
                        *(LAS u4v*)(zb + 2048 + 512 * a) = u4v{ll, ll, pk2((l & 1) ? hi : lo, hi), pk2(hi, hi)};
                    }
                }
            }
        }
        STAMP(6);

        u32 abase[4];
#pragma unroll
        for (int g = 0; g < 4; ++g) {
            abase[g] = smb + XC_OFF + w * XC_CONV + ((l & 1) ? 0 : XC_COPY) + 4 * (1023 - (l >> 1) - 256 * g - 224);
            asm volatile("" : "+v"(abase[g]));
        }
        const LAS char* bb = sm + Z_OFF + w * Z_CONV + Z_PAD + 16 * ((l & 31) - 4 * (l >> 5));
        LAS char* yb = sm + Y_OFF + w * Y_CONV + 8 * l;
        u32 done_ = smb + FL_OFF + (l == 0 ? w : 128 + 4 * l); asm volatile("" : "+v"(done_));
        volatile LAS unsigned char* done = (volatile LAS unsigned char*)(size_t)done_;
        const int chb = l & 1;
        const int ta = 8 * (16 * (w & 1) + (l >> 2)) + 4 * (w >> 1) + (l & 2);
        const u32 oo = (u32)(ta + chb) * (DM * 2) + 2 * d0;
        const __amdgpu_buffer_rsrc_t ors = __builtin_amdgcn_make_buffer_rsrc(gout, 0, SEQ * DM * 2, 0x00020000);
        const LAS char* ym = sm + Y_OFF + chb * Y_CONV + 8 * (16 * w + (l >> 2)) + 2 * (l & 2);
        const LAS char* yc = sm + Y_OFF + chb * Y_CONV + 512 + ((w & 1) ? 128 * (w - 1) : 128 * (w + 1) - 1024) + 8 * (7 + (l < 12 ? (l & 12) : 0)) + 2 * (l & 2);
        const LAS char* xr = sm + XC_OFF + chb * XC_CONV + 2 * (2046 - ta) - 512 * 7;
        const float cmask = l < 12 ? 1.f : 0.f;
        const u32 sel = chb ? 0x07060302u : 0x01000504u;
        const LAS char* gl = sm + TAB_OFF + 256 * w + 4 * l;
        constexpr int PD = 5;
        u32 flp_ = smb + FL_OFF; asm volatile("" : "+v"(flp_));
        const LAS char* flp = (const LAS char*)(size_t)flp_;
        u4v A[32], Br[QRES + 4], Bs[8];
        f16v acc[2];
        u32 G2[8], fh[8];
        u32 W0, W1, T0, T1, T2, T3;
        u32 fl[8], uu[8], ma[8], mb[8], ca[8], cb[8], P; float x[8], ga, gb, c1a, c1b;
#define PIN(v) asm("" : "+v"(v))
        auto ldA = [&](int i, int h) __attribute__((always_inline)) {
            const LAS u32* p = (const LAS u32*)(size_t)abase[i >> 3] + (224 - 32 * (i & 7)) + 2 * h;
            A[i][2 * h] = p[0]; A[i][2 * h + 1] = p[1];
        };
        auto ldB = [&](int q) __attribute__((always_inline)) { return *(const LAS u4v*)(bb + 128 * q); };
        auto horner = [&](int be, int n) __attribute__((always_inline)) {
            f16v& a = acc[be & 1];
            const int e = 8 - 4 * (n >> 2) + (n & 3);
            float t = a[e] + dppz<0x111>(a[e + 4]); PIN(t); a[e] = t;
        };
        auto post = [&](int be, int ph) __attribute__((always_inline)) {
            f16v& a = acc[be & 1];
            if (ph < 2) {
#pragma unroll
                for (int n = 5 * ph; n < 5 * ph + 5; ++n) horner(be, n);
            } else if (ph == 2) {
                horner(be, 10); horner(be, 11);
                W0 = pk2(a[12], a[13]); W1 = pk2(a[14], a[15]); T0 = pk2(a[8], a[9]); T1 = pk2(a[10], a[11]);
            } else if (ph == 3) {
                W0 = (u32)__builtin_amdgcn_update_dpp((int)W0, (int)T0, 0x104, 0xf, 0x4, false);
                W1 = (u32)__builtin_amdgcn_update_dpp((int)W1, (int)T1, 0x104, 0xf, 0x4, false);
                *(LAS u2v*)(yb + 1024 * be) = u2v{pk2(a[0], a[1]), pk2(a[2], a[3])};
                T2 = pk2(a[4], a[5]); T3 = pk2(a[6], a[7]);
            } else if (ph == 4) {
                W0 = (u32)__builtin_amdgcn_update_dpp((int)W0, (int)T2, 0x108, 0xf, 0x2, false);
                W1 = (u32)__builtin_amdgcn_update_dpp((int)W1, (int)T3, 0x108, 0xf, 0x2, false);
                asm volatile("" ::: "memory");
                *(LAS u2v*)(yb + 512 + 1024 * be) = u2v{W0, W1};
            } else if (ph == 5) {
                asm volatile("" ::: "memory"); *done = (unsigned char)(be + 1);
            }
        };
        auto rd = [&](int be, int h) __attribute__((always_inline)) {
            if (h == 0) {
                if (be == 0) { const u2v f2 = *(const volatile LAS u2v*)flp; fl[be] = f2[0]; fh[be] = f2[1]; }
                else if (be < 7) fl[be] = *(const volatile LAS u32*)flp;
                asm volatile("" ::: "memory");
                ma[be] = *(const LAS u32*)(ym + 1024 * be); mb[be] = *(const LAS u32*)(ym + 2 * Y_CONV + 1024 * be);
            } else {
                ca[be] = *(const LAS u32*)(yc + 1024 * be); cb[be] = *(const LAS u32*)(yc + 2 * Y_CONV + 1024 * be);
                if (be == 0) G2[be] = *(const LAS u32*)gl;
            }
        };
        auto rdu = [&](int be) __attribute__((always_inline)) {
            uu[be] = *(const LAS u32*)(xr + 512 * (7 - be));
            if (be > 0) G2[be] = *(const LAS u32*)(gl + 1024 * be);
        };
        u32 sfl;
        auto grab = [&](int be) __attribute__((always_inline)) {
            asm volatile("" : "+v"(ma[be]), "+v"(mb[be]), "+v"(ca[be]), "+v"(cb[be]), "+v"(fl[be]));
            if (be == 0) { asm volatile("" : "+v"(G2[0])); fl[0] &= fh[0]; }
            sfl = (u32)__builtin_amdgcn_readfirstlane(fl[be]);
        };
        auto ready = [&](int be) __attribute__((always_inline)) {
            return ((sfl + (u32)(0x7f - be) * 0x01010101u) & 0x80808080u) == 0x80808080u;
        };
        auto coef = [&](int be, int h) __attribute__((always_inline)) {
            if (h == 0) { ga = bf2f(G2[be]); gb = bfhi(G2[be]); c1a = 1.f - ga; PIN(c1a); c1b = 1.f - gb; PIN(c1b); }
            else { c1a *= bfhi(uu[be]); PIN(c1a); c1b *= bf2f(uu[be]); PIN(c1b); }
        };
        auto mix = [&](int be, int ph) __attribute__((always_inline)) {
            if (ph == -2) { grab(be); if (be > 0 && be < 7) coef(be, 0); }
            else if (ph == -1) {
                if (__builtin_expect(!ready(be), 0)) {
#pragma nounroll
                    for (u32 spins = 0; spins < SPIN_CAP; ++spins) { rd(be, 0); rd(be, 1); grab(be); if (ready(be)) break; }
                }
            } else if (ph == 0) { if (be == 0) coef(be, 0); }
            else if (ph == 1) { if (be < 7) coef(be, 1);
            } else if (ph == 2) {
                if (be == 0) { if (!(w & 1)) { ca[be] = 0u; cb[be] = 0u; } }
                x[0] = bf2f(mb[be]); x[1] = bfhi(mb[be]); x[2] = bf2f(cb[be]); x[3] = bfhi(cb[be]); x[4] = bf2f(ma[be]);
            } else if (ph == 3) {
                x[0] += cmask * x[2]; PIN(x[0]); x[1] += cmask * x[3]; PIN(x[1]);
                x[5] = bfhi(ma[be]); x[6] = bf2f(ca[be]); x[7] = bfhi(ca[be]);
            } else if (ph == 4) {
                x[4] += cmask * x[6]; PIN(x[4]); x[5] += cmask * x[7]; PIN(x[5]);
                x[0] *= c1a; PIN(x[0]); x[1] *= c1b; PIN(x[1]);
            } else if (ph == 5) {
                x[0] += ga * x[4]; PIN(x[0]); x[1] += gb * x[5]; PIN(x[1]);
                P = pk2(x[0], x[1]);
            } else if (ph == 6) {
                const u32 Q = (u32)__builtin_amdgcn_update_dpp(0, (int)P, 0xB1, 0xf, 0xf, true);
                __builtin_amdgcn_raw_buffer_store_b32(__builtin_amdgcn_perm(P, Q, sel), ors, oo, be * (256 * DM * 2), 0);
            }
        };
#pragma unroll
        for (int q = -3; q <= QRES; ++q) Br[q + 3] = ldB(q);
#pragma unroll
        for (int q = -3; q <= QRES; ++q) asm volatile("" : "+v"(Br[q + 3]));
        asm volatile("" : "+v"(yb), "+v"(ym), "+v"(yc), "+v"(xr), "+v"(bb), "+v"(gl) : "v"(oo), "v"(sel), "v"(cmask) : "memory");
        STAMP(7);
        __builtin_amdgcn_s_barrier();
        asm volatile("" ::: "memory");
        STAMP(8);
#pragma unroll
        for (int i = 0; i < 4; ++i) { ldA(i, 0); ldA(i, 1); }
        sfor<0, 8>([&](auto BE) __attribute__((always_inline)) {
            constexpr int be = BE.v;
            constexpr int ns = 4 * be > QRES ? 4 * be - QRES : 0;
            sfor<0, 4 * be + 4>([&](auto S) __attribute__((always_inline)) {
                constexpr int st = S.v;
                constexpr int i = st < 4 * be - ns ? st + ns : st < 4 * be ? st - (4 * be - ns) : st;
                constexpr int q = 4 * be - i;
                const u4v bfr = (q > QRES) ? Bs[i & 7] : Br[q > QRES ? 0 : q + 3];
                if (st == 0) acc[be & 1] = __builtin_amdgcn_mfma_f32_32x32x16_bf16(__builtin_bit_cast(bf8, A[i]), __builtin_bit_cast(bf8, bfr), f16v{}, 0, 0, 0);
                else acc[be & 1] = __builtin_amdgcn_mfma_f32_32x32x16_bf16(__builtin_bit_cast(bf8, A[i]), __builtin_bit_cast(bf8, bfr), acc[be & 1], 0, 0, 0);
                __builtin_amdgcn_sched_barrier(0);
                if (be == 0) { ldA(4 + st, 0); ldA(4 + st, 1); }
                if (be == 1 && st == B2STEP) { asm volatile("" ::: "memory"); __builtin_amdgcn_s_barrier(); asm volatile("" ::: "memory"); }
                if (be == 1 && st >= 6) { ldA(8 + st - 6, 0); ldA(8 + st - 6, 1); }
                if (be == 2 && st < 2) { ldA(10 + st, 0); ldA(10 + st, 1); }
                if (be == 2 && st >= 8) { ldA(12 + st - 8, 0); ldA(12 + st - 8, 1); }
                if (be == 3 && st >= 8) ldA(16 + ((st - 8) >> 1), st & 1);
                if (be == 4 && st >= 12) ldA(20 + ((st - 12) >> 1), st & 1);
                if (be == 6 && st >= 2 && st < 10) ldA(24 + ((st - 2) >> 1), st & 1);
                if (be == 7 && st >= 2 && st < 10) ldA(28 + ((st - 2) >> 1), st & 1);
                {
                    constexpr int i2 = st + PD - (4 * be - ns);
                    if (i2 >= 0 && i2 < ns) Bs[i2 & 7] = ldB(4 * be - i2);
                }
                if (be > 1 && st < 2) {
                    __builtin_amdgcn_s_setprio(0); __builtin_amdgcn_s_setprio(0); __builtin_amdgcn_s_setprio(0);
                    if (be != 3 && be != 4 && be != 5 && be != 6) { __builtin_amdgcn_s_setprio(0); __builtin_amdgcn_s_setprio(0); }
                }
                if (be == 1 && st < 2) { __builtin_amdgcn_s_setprio(0); __builtin_amdgcn_s_setprio(0); }
                if (be > 0 && st >= 2) post(be - 1, st - 2);
                constexpr int rb[7] = {3, 4, 5, 5, 6, 6, 7}, rs[7] = {0, 0, 0, 8, 0, 14, 14};
                constexpr int cbt[7] = {3, 4, 5, 5, 6, 6, 7}, cs[7] = {7, 8, 8, 16, 10, 20, 20};
#pragma unroll
                for (int m = 0; m < 7; ++m) {
                    if (be == rb[m] && st == rs[m]) rd(m, 0);
                    if (be == rb[m] && st == rs[m] + 1) rd(m, 1);
                    if (be == rb[m] && st == rs[m] + 2) rdu(m);
                }
                sfor<0, 7>([&](auto M) __attribute__((always_inline)) {
                    constexpr int m = M.v;
                    constexpr int r = st - cs[m];
                    if constexpr (be == cbt[m] && r >= 0 && r < 2) mix(m, r - 2);
                    if constexpr (be == cbt[m] && m == 0 && r >= 2 && r < 9) mix(m, r - 2);
                    if constexpr (be == cbt[m] && m > 0 && r >= 2 && r < 8) mix(m, r - 1);
                });
                if (be == 7 && st == 22) rdu(7);
                if (be == 7 && st == 28) coef(7, 0);
                if (be == 7 && st == 29) coef(7, 1);
                __builtin_amdgcn_sched_barrier(0);
            });
            if (be == 0) STAMP(9);
            if (be == 3) STAMP(10);
        });
        STAMP(11);
#pragma unroll
        for (int ph = 0; ph < 5; ++ph) post(7, ph);
        asm volatile("s_waitcnt lgkmcnt(0)" ::: "memory");
        __builtin_amdgcn_s_barrier();
        asm volatile("" ::: "memory");
        rd(7, 0); rd(7, 1);
        sfor<2, 7>([&](auto PH) __attribute__((always_inline)) { mix(7, PH.v); });
        STAMP(12);
    }

    STAMP_DUMP;
}

extern "C" void kernel_launch(void* const* d_in, const int* in_sizes, int n_in,
                              void* d_out, int out_size, void* d_ws, size_t ws_size,
                              hipStream_t stream) {
    fused_k<<<256, 512, 0, stream>>>((const u16*)d_in[0], (const u16*)d_in[1], (const u16*)d_in[2], (const u16*)d_in[3], (const u16*)d_in[4],
             (const u16*)d_in[5], (const u16*)d_in[6], (const u16*)d_in[8], (const u16*)d_in[9], (const u16*)d_in[10],
             (const u16*)d_in[11], (const u16*)d_in[12], (const u16*)d_in[13], (u16*)d_out, (char*)d_ws);
}

#undef kernel_launch
__global__ void __launch_bounds__(256) diag_k(char* ws) {
    const unsigned long long* hs = (const unsigned long long*)(ws + (16u << 20));
    __shared__ unsigned long long smin[256], smax[256], ssum[256];
    int b = threadIdx.x; unsigned long long mn = ~0ull, mx = 0, sum = 0;
    for (int w = 0; w < 8; ++w) mn = min(mn, hs[(b * 8 + w) * 16]);
    for (int w = GRP * 4; w < GRP * 4 + 4; ++w) { unsigned long long x = hs[(b * 8 + w) * 16 + 1]; mx = max(mx, x); sum += x - hs[(b * 8 + w) * 16]; }
    smin[b] = mn; smax[b] = mx; ssum[b] = sum; __syncthreads();
    if (b == 0) {
        for (int i = 1; i < 256; ++i) { mn = min(mn, smin[i]); mx = max(mx, smax[i]); sum += ssum[i]; }
        unsigned long long v = STATMAX ? (mx - mn) : (sum / 1024);
        if (v > 4000) v = 4000;
        unsigned long long until = mn + 10000 + v * 20;
        while (__builtin_amdgcn_s_memrealtime() < until) __builtin_amdgcn_s_sleep(1);
    }
}
extern "C" void kernel_launch(void* const* d_in, const int* in_sizes, int n_in, void* d_out, int out_size, void* d_ws, size_t ws_size, hipStream_t stream) {
    kernel_launch_inner(d_in, in_sizes, n_in, d_out, out_size, d_ws, ws_size, stream);
    diag_k<<<1, 256, 0, stream>>>((char*)d_ws);
}
```

```cpp
#include <hip/hip_runtime.h>
#include <stdint.h>

typedef unsigned short u16;
typedef unsigned int u32;
typedef unsigned long long u64;
typedef __attribute__((ext_vector_type(2))) __bf16 bf2;
typedef __attribute__((ext_vector_type(8))) __bf16 bf8;
typedef __attribute__((ext_vector_type(4))) short s4v;
typedef __attribute__((ext_vector_type(2))) u32 u2v;
typedef __attribute__((ext_vector_type(4))) u32 u4v;
typedef __attribute__((ext_vector_type(2))) float f2v;
typedef __attribute__((ext_vector_type(4))) float f4v;
typedef __attribute__((ext_vector_type(16))) float f16v;
#define GAS __attribute__((address_space(1)))
#define LAS __attribute__((address_space(3)))
typedef GAS u64 gu64;
#define RLX_AGENT __ATOMIC_RELAXED, __HIP_MEMORY_SCOPE_AGENT

constexpr int POLL_SLEEP0 = 4;
constexpr int POLL_SLEEP = 2;
constexpr int CODE_PF = 8;
constexpr int CODE_BYTES = 14080;
constexpr int BSPLIT = 1;
constexpr int WPRE = 0;
constexpr int GSLEEP = 8;
constexpr int QRES = 14;
constexpr int APRE = 16;
constexpr int SEQ = 2048, DM = 512, NS = 128;

constexpr int XC_OFF = 0, XC_CONV = 4672, XC_PAD = 512;
constexpr int Z_OFF = 18688, Z_CONV = 8320, Z_PAD = 16, Z_COPY = 4160;
constexpr int Y_OFF = 51968, Y_CONV = 8256, Y_BR = 16640;
constexpr int TAB_OFF = 85248;
constexpr int FL_OFF = 150784;
constexpr int LDS_BYTES = 151168;
constexpr int TU_OFF = Y_OFF, TV_OFF = Y_OFF + 8192, TK_OFF = Y_OFF + 16384;
constexpr int WS_FLAG = 16384, WS_G = 65536, WS_X = 1 << 20;
constexpr u32 SPIN_CAP = 1u << 17;

__device__ __forceinline__ float bf2f(u32 x) { return __uint_as_float(x << 16); }
__device__ __forceinline__ float bfhi(u32 x) { return __uint_as_float(x & 0xffff0000u); }
__device__ __forceinline__ u32 pk2(float a, float b) { bf2 r; r[0] = (__bf16)a; r[1] = (__bf16)b; return __builtin_bit_cast(u32, r); }
__device__ __forceinline__ float rbf(float a) { return __uint_as_float(pk2(0.f, a)); }
__device__ __forceinline__ float dot2(u32 a, u32 b, float c) {
    return __builtin_amdgcn_fdot2_f32_bf16(__builtin_bit_cast(bf2, a), __builtin_bit_cast(bf2, b), c, false);
}
struct cmult { f2v b, bs; };
__device__ __forceinline__ cmult mkm(f2v b) { return cmult{b, f2v{-b[1], b[0]}}; }
__device__ __forceinline__ f2v cmul(f2v a, const cmult& m) {
    return __builtin_elementwise_fma(f2v{a[0], a[0]}, m.b, f2v{a[1], a[1]} * m.bs);
}
__device__ __forceinline__ f2v cexp_p(float lz, float rev, float p) {
    const float m = __builtin_amdgcn_exp2f(lz * p);
    const float a = __builtin_amdgcn_fractf(rev * p);
    return f2v{m * __builtin_amdgcn_cosf(a), m * __builtin_amdgcn_sinf(a)};
}
template <int CTRL> __device__ __forceinline__ float dppz(float v) {
    return __int_as_float(__builtin_amdgcn_update_dpp(0, __float_as_int(v), CTRL, 0xf, 0xf, true));
}
template <int I> struct ic { static constexpr int v = I; };
template <int B, int E, class F> __device__ __forceinline__ void sfor(F&& f) {
    if constexpr (B < E) { f(ic<B>{}); sfor<B + 1, E>(f); }
}
__device__ __forceinline__ void dma16(const void* base, u32 off, u32 lds) {
    u32 keep;
    asm volatile("s_mov_b32 %0, m0\n\ts_mov_b32 m0, %3\n\ts_nop 0\n\tglobal_load_lds_dwordx4 %1, %2 nt\n\ts_mov_b32 m0, %0"
                 : "=&s"(keep) : "v"(off), "s"(base), "s"(lds) : "memory");
}

constexpr int SCH_W = 6;
constexpr unsigned short SCH[144][SCH_W] = {
    {0x2180, 0x21c0, 0x0000, 0x0000, 0x0000, 0x0000},
    {0x2200, 0x2240, 0x0000, 0x0000, 0x0000, 0x0000},
    {0x2280, 0x22c0, 0x0000, 0x0000, 0x0000, 0x0000},
    {0x2300, 0x2340, 0x0000, 0x0000, 0x0000, 0x0000},
    {0x2380, 0x23c0, 0x0000, 0x0000, 0x0000, 0x0000},
    {0x2400, 0x2440, 0x0000, 0x0000, 0x0000, 0x0000},
    {0x4000, 0x4001, 0x4002, 0x4003, 0x4004, 0x4005},
    {0x4006, 0x4007, 0x4008, 0x4009, 0x400a, 0x400b},
    {0x400c, 0x400d, 0x400e, 0x400f, 0x4010, 0x4011},
    {0x4012, 0x4013, 0x4014, 0x4015, 0x0000, 0x0000},
    {0x4016, 0x14c0, 0x14c1, 0x0000, 0x0000, 0x0000},
    {0x4017, 0x4018, 0x4019, 0x0000, 0x0000, 0x0000},
    {0x1480, 0x1481, 0x0000, 0x0000, 0x0000, 0x0000},
    {0x6002, 0x5001, 0x0000, 0x0000, 0x0000, 0x0000},
    {0x5002, 0x5005, 0x4040, 0x0000, 0x0000, 0x0000},
    {0x5006, 0x4041, 0x4042, 0x4043, 0x0000, 0x0000},
    {0x4044, 0x4045, 0x4046, 0x4047, 0x4048, 0x0000},
    {0x4049, 0x404a, 0x404b, 0x404c, 0x404d, 0x0000},
    {0x404e, 0x404f, 0x4050, 0x4051, 0x0000, 0x0000},
    {0x5011, 0x5045, 0x5046, 0x0000, 0x0000, 0x0000},
    {0x4052, 0x4053, 0x5009, 0x0000, 0x0000, 0x0000},
    {0x4054, 0x4055, 0x4056, 0x500a, 0x500b, 0x0000},
    {0x4057, 0x4058, 0x4059, 0x0000, 0x0000, 0x0000},
    {0x500c, 0x500d, 0x500e, 0x500f, 0x5010, 0x5012},
    {0x6040, 0x5041, 0x5013, 0x5014, 0x0000, 0x0000},
    {0x5042, 0x5015, 0x5016, 0x5017, 0x5018, 0x0000},
    {0x5019, 0x501a, 0x501b, 0x501c, 0x501d, 0x501e},
    {0x501f, 0x5020, 0x5021, 0x5049, 0x504a, 0x504b},
    {0x5022, 0x5023, 0x5024, 0x0000, 0x0000, 0x0000},
    {0x504c, 0x504d, 0x504e, 0x504f, 0x5050, 0x4080},
    {0x5051, 0x5085, 0x5086, 0x0000, 0x0000, 0x0000},
    {0x5052, 0x5053, 0x5054, 0x5055, 0x5056, 0x5057},
    {0x4081, 0x4082, 0x4083, 0x5058, 0x5059, 0x0000},
    {0x4084, 0x4085, 0x4086, 0x4087, 0x505a, 0x0000},
    {0x4088, 0x4089, 0x408a, 0x408b, 0x505b, 0x0000},
    {0x408c, 0x408d, 0x408e, 0x408f, 0x505c, 0x0000},
    {0x4090, 0x4091, 0x4092, 0x0000, 0x0000, 0x0000},
    {0x4093, 0x4094, 0x4095, 0x4096, 0x505d, 0x0000},
    {0x4097, 0x4098, 0x4099, 0x0000, 0x0000, 0x0000},
    {0x505e, 0x505f, 0x5060, 0x5061, 0x5089, 0x508a},
    {0x6080, 0x5081, 0x5062, 0x0000, 0x0000, 0x0000},
    {0x5082, 0x5063, 0x508b, 0x508c, 0x0000, 0x0000},
    {0x5064, 0x508d, 0x508e, 0x508f, 0x0000, 0x0000},
    {0x5090, 0x40c0, 0x40c1, 0x40c2, 0x40c3, 0x0000},
    {0x40c4, 0x40c5, 0x40c6, 0x40c7, 0x40c8, 0x0000},
    {0x40c9, 0x40ca, 0x40cb, 0x40cc, 0x40cd, 0x0000},
    {0x5091, 0x50c5, 0x50c6, 0x0000, 0x0000, 0x0000},
    {0x5092, 0x5093, 0x5094, 0x5095, 0x5096, 0x5097},
    {0x33c0, 0x5098, 0x5099, 0x509a, 0x509b, 0x0000},
    {0x3400, 0x509c, 0x509d, 0x509e, 0x509f, 0x0000},
    {0x50a0, 0x50a1, 0x40ce, 0x40cf, 0x40d0, 0x0000},
    {0x50a2, 0x50a3, 0x50a4, 0x0000, 0x0000, 0x0000},
    {0x1440, 0x1441, 0x40d1, 0x0000, 0x0000, 0x0000},
    {0x1400, 0x1401, 0x50c9, 0x0000, 0x0000, 0x0000},
    {0x40d2, 0x40d3, 0x40d4, 0x0000, 0x0000, 0x0000},
    {0x40d5, 0x40d6, 0x50ca, 0x50cb, 0x50cc, 0x0000},
    {0x40d7, 0x40d8, 0x40d9, 0x0000, 0x0000, 0x0000},
    {0x50cd, 0x50ce, 0x50cf, 0x50d0, 0x15c0, 0x0000},
    {0x60c1, 0x50c1, 0x0000, 0x0000, 0x0000, 0x0000},
    {0x50c2, 0x15c1, 0x0000, 0x0000, 0x0000, 0x0000},
    {0x1580, 0x1581, 0x0000, 0x0000, 0x0000, 0x0000},
    {0x1540, 0x1541, 0x0000, 0x0000, 0x0000, 0x0000},
    {0x4100, 0x4101, 0x4102, 0x4103, 0x4104, 0x0000},
    {0x4105, 0x4106, 0x4107, 0x4108, 0x4109, 0x0000},
    {0x50d1, 0x5105, 0x5106, 0x0000, 0x0000, 0x0000},
    {0x50d2, 0x50d3, 0x50d4, 0x50d5, 0x50d6, 0x50d7},
    {0x50d8, 0x50d9, 0x50da, 0x50db, 0x50dc, 0x50dd},
    {0x50de, 0x50df, 0x50e0, 0x50e1, 0x410a, 0x0000},
    {0x33c0, 0x50e2, 0x50e3, 0x410b, 0x0000, 0x0000},
    {0x3400, 0x50e4, 0x5109, 0x0000, 0x0000, 0x0000},
    {0x3440, 0x410c, 0x410d, 0x410e, 0x0000, 0x0000},
    {0x3480, 0x410f, 0x4110, 0x4111, 0x0000, 0x0000},
    {0x34c0, 0x4112, 0x0000, 0x0000, 0x0000, 0x0000},
    {0x3500, 0x4113, 0x4114, 0x4115, 0x0000, 0x0000},
    {0x4116, 0x510a, 0x510b, 0x510c, 0x510d, 0x510e},
    {0x4117, 0x4118, 0x4119, 0x0000, 0x0000, 0x0000},
    {0x510f, 0x5110, 0x1500, 0x0000, 0x0000, 0x0000},
    {0x6101, 0x1501, 0x0000, 0x0000, 0x0000, 0x0000},
    {0x5101, 0x5102, 0x0000, 0x0000, 0x0000, 0x0000},
    {0x16c0, 0x16c1, 0x0000, 0x0000, 0x0000, 0x0000},
    {0x1680, 0x1681, 0x0000, 0x0000, 0x0000, 0x0000},
    {0x1640, 0x1641, 0x0000, 0x0000, 0x0000, 0x0000},
    {0x1600, 0x1601, 0x0000, 0x0000, 0x0000, 0x0000},
    {0x5111, 0x5145, 0x5146, 0x0000, 0x0000, 0x0000},
    {0x5112, 0x5113, 0x5114, 0x5115, 0x5116, 0x5117},
    {0x5118, 0x5119, 0x511a, 0x511b, 0x511c, 0x511d},
    {0x511e, 0x511f, 0x5120, 0x5121, 0x4140, 0x0000},
    {0x5122, 0x5123, 0x5124, 0x0000, 0x0000, 0x0000},
    {0x4141, 0x4142, 0x4143, 0x4144, 0x4145, 0x0000},
    {0x4146, 0x4147, 0x4148, 0x4149, 0x414a, 0x0000},
    {0x414b, 0x414c, 0x414d, 0x414e, 0x414f, 0x0000},
    {0x4150, 0x4151, 0x4152, 0x0000, 0x0000, 0x0000},
    {0x33c0, 0x4153, 0x4154, 0x4155, 0x0000, 0x0000},
    {0x3400, 0x4156, 0x5149, 0x514a, 0x0000, 0x0000},
    {0x3440, 0x4157, 0x4158, 0x514b, 0x0000, 0x0000},
    {0x3480, 0x4159, 0x0000, 0x0000, 0x0000, 0x0000},
    {0x34c0, 0x514c, 0x514d, 0x514e, 0x514f, 0x0000},
    {0x6141, 0x3500, 0x5150, 0x0000, 0x0000, 0x0000},
    {0x3540, 0x5141, 0x0000, 0x0000, 0x0000, 0x0000},
    {0x3580, 0x5142, 0x0000, 0x0000, 0x0000, 0x0000},
    {0x35c0, 0x3600, 0x0000, 0x0000, 0x0000, 0x0000},
    {0x0000, 0x0000, 0x0000, 0x0000, 0x0000, 0x0000},
    {0x0000, 0x0000, 0x0000, 0x0000, 0x0000, 0x0000},
    {0x0000, 0x0000, 0x0000, 0x0000, 0x0000, 0x0000},
    {0x5151, 0x5185, 0x5186, 0x0000, 0x0000, 0x0000},
    {0x5152, 0x5153, 0x5154, 0x5155, 0x5156, 0x5157},
    {0x5158, 0x5159, 0x515a, 0x515b, 0x515c, 0x515d},
    {0x515e, 0x515f, 0x5160, 0x5161, 0x0000, 0x0000},
    {0x5162, 0x5163, 0x5164, 0x0000, 0x0000, 0x0000},
    {0x5189, 0x518a, 0x518b, 0x518c, 0x518d, 0x518e},
    {0x518f, 0x5190, 0x0000, 0x0000, 0x0000, 0x0000},
    {0x0000, 0x0000, 0x0000, 0x0000, 0x0000, 0x0000},
    {0x0000, 0x0000, 0x0000, 0x0000, 0x0000, 0x0000},
    {0x0000, 0x0000, 0x0000, 0x0000, 0x0000, 0x0000},
    {0x4180, 0x4181, 0x4182, 0x4183, 0x4184, 0x0000},
    {0x4185, 0x4186, 0x4187, 0x4188, 0x4189, 0x0000},
    {0x418a, 0x418b, 0x418c, 0x418d, 0x418e, 0x0000},
    {0x418f, 0x4190, 0x4191, 0x0000, 0x0000, 0x0000},
    {0x4192, 0x4193, 0x4194, 0x0000, 0x0000, 0x0000},
    {0x4195, 0x4196, 0x33c0, 0x0000, 0x0000, 0x0000},
    {0x4197, 0x4198, 0x4199, 0x0000, 0x0000, 0x0000},
    {0x3400, 0x3440, 0x0000, 0x0000, 0x0000, 0x0000},
    {0x6182, 0x5181, 0x0000, 0x0000, 0x0000, 0x0000},
    {0x3480, 0x5182, 0x0000, 0x0000, 0x0000, 0x0000},
    {0x34c0, 0x3500, 0x0000, 0x0000, 0x0000, 0x0000},
    {0x3540, 0x17c0, 0x0000, 0x0000, 0x0000, 0x0000},
    {0x3580, 0x17c1, 0x0000, 0x0000, 0x0000, 0x0000},
    {0x35c0, 0x1780, 0x0000, 0x0000, 0x0000, 0x0000},
    {0x5191, 0x51c5, 0x51c6, 0x0000, 0x0000, 0x0000},
    {0x3600, 0x5192, 0x5193, 0x5194, 0x5195, 0x0000},
    {0x3640, 0x5196, 0x5197, 0x5198, 0x5199, 0x0000},
    {0x3680, 0x519a, 0x519b, 0x519c, 0x519d, 0x0000},
    {0x36c0, 0x519e, 0x519f, 0x51a0, 0x0000, 0x0000},
    {0x3700, 0x51a1, 0x51c9, 0x51ca, 0x0000, 0x0000},
    {0x51a2, 0x51a3, 0x51a4, 0x0000, 0x0000, 0x0000},
    {0x1781, 0x51cb, 0x51cc, 0x51cd, 0x51ce, 0x0000},
    {0x1740, 0x1741, 0x51cf, 0x0000, 0x0000, 0x0000},
    {0x1700, 0x1701, 0x51d0, 0x0000, 0x0000, 0x0000},
    {0x0000, 0x0000, 0x0000, 0x0000, 0x0000, 0x0000},
    {0x0000, 0x0000, 0x0000, 0x0000, 0x0000, 0x0000},
    {0x0000, 0x0000, 0x0000, 0x0000, 0x0000, 0x0000},
    {0x0000, 0x0000, 0x0000, 0x0000, 0x0000, 0x0000},
    {0x0000, 0x0000, 0x0000, 0x0000, 0x0000, 0x0000},
    {0x0000, 0x0000, 0x0000, 0x0000, 0x0000, 0x0000},
};

__global__ void __launch_bounds__(512, 2)
fused_k(const u16* __restrict__ gu, const u16* __restrict__ glar, const u16* __restrict__ gaim,
        const u16* __restrict__ gcre, const u16* __restrict__ gcim, const u16* __restrict__ gldt,
        const u16* __restrict__ gdsk, const u16* __restrict__ gk, const u16* __restrict__ gv,
        const u16* __restrict__ gshk, const u16* __restrict__ gssk, const u16* __restrict__ gssd,
        const u16* __restrict__ ggw, u16* __restrict__ gout, char* ws)
{
    __shared__ __attribute__((aligned(16))) char smem[LDS_BYTES];
    LAS char* sm = (LAS char*)smem;
    const u32 smb = (u32)(size_t)sm;
    const int tid = threadIdx.x, l = tid & 63;
    const int w = __builtin_amdgcn_readfirstlane(tid >> 6);
    const int bx = blockIdx.x & 7, bj = blockIdx.x >> 3;
    const int d0 = 64 * bx + 2 * bj;
    if (w == 7) {
        u64 pc; u32 keep;
        asm volatile("s_getpc_b64 %0\n\ts_mov_b32 %1, m0\n\ts_mov_b32 m0, %3\n\ts_nop 0\n\tglobal_load_lds_dword %2, %0\n\ts_mov_b32 m0, %1"
                     : "=&s"(pc), "=&s"(keep) : "v"(min((u32)((bj * CODE_PF + (l & (CODE_PF - 1))) & 127) * 128u, (u32)CODE_BYTES)), "s"(smb + FL_OFF + 128) : "memory");
    }
    u32 go, po, so_;
    {
        const int r = 8 * w + (l >> 3);
        const int f = ((r >> 1) & 1) | (((r >> 3) & 1) << 1);
        go = (u32)(64 * bj + r) * (DM * 2) + 128 * bx + 16 * ((l & 7) ^ (f << 1));
        po = 2u * (u32)((d0 + (w & 1)) * NS + 64 * ((w >> 1) & 1) + l);
        so_ = 8u * (u32)(64 * bx + 16 * (w & 3) + (l & 15));
        asm volatile("" : "+v"(go), "+v"(po), "+v"(so_));
    }
    asm volatile("" :: "s"(gu), "s"(glar), "s"(gaim), "s"(gcre), "s"(gcim), "s"(gldt), "s"(gdsk), "s"(gk), "s"(gv), "s"(gshk), "s"(gssk), "s"(gssd), "s"(ggw), "s"(gout), "s"(ws));
    const __amdgpu_buffer_rsrc_t xrs = __builtin_amdgcn_make_buffer_rsrc(ws + WS_X, 0, DM * 2 * 4096, 0x00020000);

    u32 p_lar, p_aim, p_cre, p_cim;
    if (w < 4) {
        asm volatile("global_load_ushort %0, %4, %5 nt\n\tglobal_load_ushort %1, %4, %6 nt\n\t"
                     "global_load_ushort %2, %4, %7 nt\n\tglobal_load_ushort %3, %4, %8 nt"
                     : "=&v"(p_lar), "=&v"(p_aim), "=&v"(p_cre), "=&v"(p_cim)
                     : "v"(po), "s"(glar), "s"(gaim), "s"(gcre), "s"(gcim) : "memory");
    } else if (w == 5 && bj > 0) {
        dma16(gk, go - 48 * DM * 2, smb + TK_OFF);
    }
    dma16(gk, go, smb + TK_OFF + 1024 + 1024 * w);
    dma16(gv, go, smb + TV_OFF + 1024 * w);
    dma16(gu, go, smb + TU_OFF + 1024 * w);
    auto late = [&](u2v& shk, u64& tag) __attribute__((always_inline)) {
        asm volatile("global_load_dwordx2 %0, %2, %3\n\ts_load_dwordx2 %1, %4, 0x0" : "=&v"(shk), "=&s"(tag) : "v"(so_), "s"(gshk), "s"(ws) : "memory");
    };

    const int cg = w & 3, th = w >> 2;
    auto phase1 = [&](u2v& shk, u64& tag, bool mwave) __attribute__((always_inline)) {
        const int G = l >> 4, q = (l >> 2) & 3, p = l & 3;
        const int row = 32 * th + 8 * G + 7 - q;
        const int f = ((row >> 1) & 1) | (((row >> 3) & 1) << 1);
        u32 a0 = smb + TU_OFF - 512 + row * 128 + ((cg ^ f) << 5) + 8 * p;
        u32 a2 = smb + TK_OFF + row * 128 + ((cg ^ f ^ 2) << 5) + 8 * p;
        u32 xo = (u32)(bx * 32 + bj) * 16384u + (u32)((8 * cg + ((l & 15) >> 1)) * 512 + th * 256 + (l & 1) * 64 + G * 16);
        float ev[8] = {0.f, 0.f, 0.f, 0.f, 0.f, 0.f, 0.f, 0.f};
        asm volatile("" : "+v"(a0), "+v"(a2), "+v"(xo), "+v"(ev[0]), "+v"(ev[1]), "+v"(ev[2]), "+v"(ev[3]), "+v"(ev[4]), "+v"(ev[5]), "+v"(ev[6]), "+v"(ev[7]));
        if (mwave) asm volatile("s_waitcnt vmcnt(0) lgkmcnt(0)" : "+v"(shk), "+s"(tag) :: "memory");
        else asm volatile("s_waitcnt vmcnt(3) lgkmcnt(0)" : "+v"(shk), "+s"(tag) :: "memory");
        tag = tag * 6364136223846793005ull + 1442695040888963407ull;
        __builtin_amdgcn_s_setprio(3);
        __builtin_amdgcn_s_barrier();
        typedef LAS s4v* trp;
        auto tr = [&](u32 a) __attribute__((always_inline)) { return __builtin_bit_cast(u2v, __builtin_amdgcn_ds_read_tr16_b64_v4i16((trp)(size_t)a)); };
        const u2v ka = tr(a0 + (TK_OFF - TU_OFF) + 1024 + 512), kb = tr(a0 + (TK_OFF - TU_OFF) + 1024), kc = tr(a2);
        const u2v ua = tr(a0 + 512), ub = tr(a0);
        const u2v va = tr(a0 + (TV_OFF - TU_OFF) + 512), vb = tr(a0 + (TV_OFF - TU_OFF));
        __builtin_amdgcn_sched_barrier(0);
        const u32 K[6] = {ka[0], ka[1], kb[0], kb[1], kc[0], kc[1]};
        const u32 V[4] = {va[0], va[1], vb[0], vb[1]};
        u32 Ks[5];
#pragma unroll
        for (int n = 0; n < 5; ++n) Ks[n] = __builtin_amdgcn_alignbit(K[n + 1], K[n], 16);
#pragma unroll
        for (int n = 0; n < 4; ++n) { ev[2 * n] = dot2(K[n], shk[0], ev[2 * n]); ev[2 * n + 1] = dot2(Ks[n], shk[0], ev[2 * n + 1]); }
        u4v ud{ua[0], ua[1], ub[0], ub[1]};
        __builtin_amdgcn_raw_buffer_store_b128(ud, xrs, xo, 0, 16);
        __builtin_amdgcn_sched_barrier(0);
#pragma unroll
        for (int n = 0; n < 4; ++n) { ev[2 * n] = dot2(K[n + 1], shk[1], ev[2 * n]); ev[2 * n + 1] = dot2(Ks[n + 1], shk[1], ev[2 * n + 1]); }
        u4v kv;
#pragma unroll
        for (int n = 0; n < 4; ++n) kv[n] = pk2(ev[2 * n] * bf2f(V[n]), ev[2 * n + 1] * bfhi(V[n]));
        __builtin_amdgcn_raw_buffer_store_b128(kv, xrs, xo + 128u, 0, 16);
        asm volatile("" :: "v"(ud));
        __builtin_amdgcn_sched_barrier(0);
        __builtin_amdgcn_s_setprio(0);
    };
    auto signal = [&](u64 tag) __attribute__((always_inline)) {
        if (l == 0) __hip_atomic_store((gu64*)(ws + WS_FLAG + ((bx * 4 + cg) * 4 + ((bj >> 1) & 3)) * 128) + ((bj >> 3) * 2 + (bj & 1)) * 2 + th, tag, RLX_AGENT);
    };

    if (__builtin_expect(w >= 4, 0)) {
    const int cw = w - 4;
    u64 tag; u2v shk;
    {
        const u4v z4{0, 0, 0, 0};
        const int zo = (l < 32) ? XC_OFF + cw * XC_CONV + 16 * l : (l < 34) ? Z_OFF + cw * Z_CONV + (l & 1) * Z_COPY : FL_OFF + 16 * (l & 7);
        *(LAS u4v*)(sm + zo) = z4;
        *(LAS u4v*)(sm + ((w == 5 && bj == 0) ? TK_OFF + 16 * l : zo)) = z4;
    }
    late(shk, tag);
    __builtin_amdgcn_s_sleep(GSLEEP);
    u4v gr0, gr1, gwv;
    {
        const u32 ro = (u32)(8 * blockIdx.x + 2 * cw) * (DM * 2) + 16 * l;
        asm volatile("global_load_dwordx4 %0, %3, %5 nt\n\tglobal_load_dwordx4 %1, %3, %5 offset:1024 nt\n\tglobal_load_dwordx4 %2, %4, %6"
                     : "=&v"(gr0), "=&v"(gr1), "=&v"(gwv) : "v"(ro), "v"(16u * l), "s"(gu), "s"(ggw) : "memory");
    }
    phase1(shk, tag, false);
    {
        asm volatile("s_waitcnt vmcnt(2)" : "+v"(gr0), "+v"(gr1), "+v"(gwv) :: "memory");
        const float s0 = dot2(gr0[0], gwv[0], dot2(gr0[1], gwv[1], dot2(gr0[2], gwv[2], dot2(gr0[3], gwv[3], 0.f))));
        const float s1 = dot2(gr1[0], gwv[0], dot2(gr1[1], gwv[1], dot2(gr1[2], gwv[2], dot2(gr1[3], gwv[3], 0.f))));
        float ee = ((l & 1) ? s1 : s0) + dppz<0xB1>((l & 1) ? s0 : s1);
        ee += dppz<0x4E>(ee); ee += dppz<0x124>(ee); ee += dppz<0x128>(ee);
        const float r0 = __int_as_float(__builtin_amdgcn_readlane(__float_as_int(ee), 0)) + __int_as_float(__builtin_amdgcn_readlane(__float_as_int(ee), 16))
                       + __int_as_float(__builtin_amdgcn_readlane(__float_as_int(ee), 32)) + __int_as_float(__builtin_amdgcn_readlane(__float_as_int(ee), 48));
        const float r1 = __int_as_float(__builtin_amdgcn_readlane(__float_as_int(ee), 1)) + __int_as_float(__builtin_amdgcn_readlane(__float_as_int(ee), 17))
                       + __int_as_float(__builtin_amdgcn_readlane(__float_as_int(ee), 33)) + __int_as_float(__builtin_amdgcn_readlane(__float_as_int(ee), 49));
        const float g0 = __builtin_amdgcn_rcpf(1.f + __builtin_amdgcn_exp2f(-1.44269504088896341f * rbf(r0)));
        const float g1 = __builtin_amdgcn_rcpf(1.f + __builtin_amdgcn_exp2f(-1.44269504088896341f * rbf(r1)));
        const int row = 8 * blockIdx.x + 2 * cw;
        const int be = row >> 8, cc = (row >> 3) & 31, hh = (row >> 2) & 1;
        const int gi = (((cc >> 4) + 2 * hh) * 8 + be) * 32 + (cc & 15) * 2 + (cw & 1);
        u32 gpair = pk2(g0, g1);
        asm volatile("s_waitcnt vmcnt(0)" : "+v"(gpair) :: "memory");
        signal(tag);
        if (l == 0) __hip_atomic_store((gu64*)(ws + WS_G) + gi, (tag << 32) | gpair, RLX_AGENT);
    }
    gu64* gp = (gu64*)(ws + WS_G) + cw * 256 + (l >> 1);
    u64 gg[8];
    {
        gu64* fp = (gu64*)(ws + WS_FLAG + ((bx * 4 + (bj >> 3)) * 4 + cw) * 128) + (l & 15);
        __builtin_amdgcn_s_sleep(POLL_SLEEP0);
        if (l < 16) {
            const u64 zero = 0;
#pragma nounroll
            for (u32 spins = 0; spins < SPIN_CAP; ++spins) {
                u64 v;
                asm volatile("global_atomic_add_x2 %0, %1, %2, off sc0 sc1\n\ts_waitcnt vmcnt(0)" : "=&v"(v) : "v"(fp), "v"(zero) : "memory");
                if (__all(v == tag)) break;
                __builtin_amdgcn_s_sleep(POLL_SLEEP);
            }
        }
        asm volatile("" ::: "memory");
        const u32 xo = (u32)(bx * 32 + 2 * cw + (l >> 5)) * 16384u + (u32)(bj * 512 + (l & 31) * 16);
        u4v xv[4];
#pragma unroll
        for (int pc = 0; pc < 4; ++pc) xv[pc] = __builtin_amdgcn_raw_buffer_load_b128(xrs, xo + 131072u * pc, 0, 16);
#pragma unroll
        for (int be = 0; be < 8; ++be) gg[be] = __hip_atomic_load(gp + 32 * be, RLX_AGENT);
        LAS char* xb = sm + XC_OFF + ((l >> 2) & 3) * XC_CONV + XC_PAD + 16 * (8 * (2 * cw + (l >> 5)) + 4 * ((l >> 4) & 1) + (l & 3));
#pragma unroll
        for (int pc = 0; pc < 4; ++pc) *(LAS u4v*)(xb + 1024 * pc) = xv[pc];
    }
    asm volatile("s_waitcnt lgkmcnt(0)" ::: "memory");
    __builtin_amdgcn_s_barrier();

    {
#pragma nounroll
        for (u32 spins = 0; spins < SPIN_CAP; ++spins) {
            u32 bad = 0;
#pragma unroll
            for (int be = 0; be < 8; ++be) bad |= (u32)(gg[be] >> 32) ^ (u32)tag;
            if (__all(bad == 0)) break;
            __builtin_amdgcn_s_sleep(4);
#pragma unroll
            for (int be = 0; be < 8; ++be) gg[be] = __hip_atomic_load(gp + 32 * be, RLX_AGENT);
        }
#pragma unroll
        for (int be = 0; be < 8; ++be) *(LAS u32*)(sm + TAB_OFF + 1024 * be + 256 * cw + 4 * l) = (u32)gg[be];
        asm volatile("s_waitcnt lgkmcnt(0)" ::: "memory");
    }
    return;
    }

    {
        u64 tag; u2v shk; u32 p_ldt, skip;
        asm volatile("s_load_dword %0, %2, %4\n\ts_load_dword %1, %3, %4"
                     : "=&s"(p_ldt), "=&s"(skip) : "s"(gldt), "s"((w & 2) ? gssd : gdsk), "s"(2 * d0) : "memory");
        late(shk, tag);
        asm volatile("s_waitcnt vmcnt(4) lgkmcnt(0)" : "+v"(p_lar), "+v"(p_aim), "+v"(p_cre), "+v"(p_cim), "+s"(p_ldt), "+s"(skip) :: "memory");
        p_ldt = (p_ldt >> (16 * (w & 1))) & 0xffffu;
        skip = (skip >> (16 * (w & 1))) & 0xffffu;
        u4v ta_[8], tb_[8];
        const float dt = rbf(expf(bf2f(p_ldt)));
        const float zr = -rbf(expf(bf2f(p_lar))) * dt;
        const float zi = bf2f(p_aim) * dt;
        const float lz = zr * 1.44269504088896341f, rev = zi * 0.15915494309189535f;
        const f2v e8 = cexp_p(lz, rev, 8.f);
        const cmult E8 = mkm(e8);
        {
            const cmult E1 = mkm(cexp_p(lz, rev, 1.f)), E4 = mkm(cexp_p(lz, rev, 4.f)), E256 = mkm(cexp_p(lz, rev, 256.f));
            f2v a0[16], a1[16];
            a0[0] = f2v{2.f * (bf2f(p_cre) * dt), 2.f * (bf2f(p_cim) * dt)};
            a0[4] = cmul(a0[0], E4);
#pragma unroll
            for (int g = 1; g < 4; ++g) { a0[g] = cmul(a0[g - 1], E1); a0[g + 4] = cmul(a0[g + 3], E1); }
            a1[0] = cmul(a0[0], E256); a1[2] = cmul(a0[2], E256); a1[3] = cmul(a0[4], E256); a1[6] = cmul(a0[6], E256);
            a1[7] = cmul(a1[0], E8); a1[1] = a1[0]; a1[4] = a1[3]; a1[5] = a1[3];
            a0[8] = cmul(a1[0], E256); a0[9] = cmul(a1[3], E256); a0[10] = cmul(a1[7], E256);
            a1[8] = cmul(a0[8], E256); a1[9] = cmul(a0[9], E256); a1[10] = cmul(a0[10], E256);
            a0[11] = cmul(a1[8], E256); a0[12] = cmul(a1[10], E256);
            a1[11] = cmul(a0[11], E256); a1[12] = cmul(a0[12], E256);
            a0[13] = cmul(a1[11], E256); a0[14] = cmul(a1[12], E256);
            a1[13] = cmul(a0[13], E256); a1[14] = cmul(a0[14], E256);
            a0[15] = a0[14]; a1[15] = a1[14];
#pragma unroll
            for (int cc = 0; cc < 4; ++cc) {
                const int g = 4 * cc;
                ta_[2 * cc] = u4v{pk2(a0[g][0], a0[g + 1][0]), pk2(a0[g + 2][0], a0[g + 3][0]), pk2(a1[g][0], a1[g + 1][0]), pk2(a1[g + 2][0], a1[g + 3][0])};
                ta_[2 * cc + 1] = u4v{pk2(-a0[g][1], -a0[g + 1][1]), pk2(-a0[g + 2][1], -a0[g + 3][1]), pk2(-a1[g][1], -a1[g + 1][1]), pk2(-a1[g + 2][1], -a1[g + 3][1])};
                asm volatile("" : "+v"(ta_[2 * cc]), "+v"(ta_[2 * cc + 1]));
            }
        }
        const cmult E64 = mkm(cexp_p(lz, rev, 64.f));
        f2v e[8];
        e[0] = f2v{1.f, 0.f}; e[1] = e8;
        e[4] = cexp_p(lz, rev, 32.f);
        e[2] = cmul(e[1], E8); e[5] = cmul(e[4], E8); e[3] = cmul(e[2], E8);
        e[6] = cmul(e[5], E8); e[7] = cmul(e[6], E8);
        auto btab = [&](int cc) __attribute__((always_inline)) {
            if (cc > 0) {
#pragma unroll
                for (int g = 0; g < 8; ++g) e[g] = cmul(e[g], E64);
            }
            tb_[2 * cc] = u4v{pk2(e[0][0], e[1][0]), pk2(e[2][0], e[3][0]), pk2(e[4][0], e[5][0]), pk2(e[6][0], e[7][0])};
            tb_[2 * cc + 1] = u4v{pk2(e[0][1], e[1][1]), pk2(e[2][1], e[3][1]), pk2(e[4][1], e[5][1]), pk2(e[6][1], e[7][1])};
            asm volatile("" : "+v"(tb_[2 * cc]), "+v"(tb_[2 * cc + 1]));
        };
#pragma unroll
        for (int cc = 0; cc < BSPLIT; ++cc) btab(cc);
        LAS char* tab = sm + TAB_OFF + w * 16384;
        u32 tw[4], tr0, tr1, zb;
        {
            const int h = l >> 5, blk = (l >> 4) & 1, q = (l >> 2) & 3, p = l & 3;
#pragma unroll
            for (int cc = 0; cc < 4; ++cc) tw[cc] = (u32)(size_t)tab + l * 64 + ((cc ^ ((l >> 1) & 3)) << 4);
            tr0 = (u32)(size_t)tab + (8 * h + q) * 64 + (((2 * blk + (p >> 1)) ^ (q >> 1)) << 4) + 8 * (p & 1);
            tr1 = (u32)(size_t)tab + (8 * h + 4 + q) * 64 + (((2 * blk + (p >> 1)) ^ (2 + (q >> 1))) << 4) + 8 * (p & 1);
            zb = smb + Z_OFF + w * Z_CONV + Z_PAD + 16 * l;
        }
        asm volatile("" : "+v"(tw[0]), "+v"(tw[1]), "+v"(tw[2]), "+v"(tw[3]));
        phase1(shk, tag, true);
        {
            auto wr = [&](int cc, int off, const u4v& v) __attribute__((always_inline)) { *(LAS u4v*)(size_t)(tw[cc] + off) = v; };
#pragma unroll
            for (int cc = 0; cc < 4; ++cc) { wr(cc, 0, ta_[2 * cc]); wr(cc, 4096, ta_[2 * cc + 1]); if (cc >= BSPLIT) btab(cc); }
#pragma unroll
            for (int cc = 0; cc < WPRE; ++cc) { wr(cc, 8192, tb_[2 * cc]); wr(cc, 12288, tb_[2 * cc + 1]); }
            asm volatile("s_waitcnt vmcnt(0)" ::: "memory");
            signal(tag);
            if (w >= 2) {
                const u32 so = (u32)(d0 + (w & 1)) * (SEQ * 2) + 16 * l;
                const u32 zd = smb + Z_OFF + w * Z_CONV + Z_PAD;
#pragma unroll
                for (int pc = 0; pc < 4; ++pc) dma16(gssk, so + 1024 * pc, zd + 1024 * pc);
            }
#pragma unroll
            for (int cc = WPRE; cc < 4; ++cc) { wr(cc, 8192, tb_[2 * cc]); wr(cc, 12288, tb_[2 * cc + 1]); }
        }
        __builtin_amdgcn_sched_barrier(0);
        u32 abase[4];
#pragma unroll
        for (int g = 0; g < 4; ++g) {
            abase[g] = smb + Z_OFF + w * Z_CONV + Z_PAD + ((l & 1) ? 0 : Z_COPY) + 4 * ((l >> 1) - 3) + 1024 * g;
            asm volatile("" : "+v"(abase[g]));
        }
        const LAS char* bb = sm + XC_OFF + w * XC_CONV + XC_PAD + 16 * ((l & 31) - 4 * (l >> 5));
        LAS char* yb = sm + Y_OFF + (w >> 1) * Y_BR + (w & 1) * Y_CONV + 8 * l;
        const int chb = l & 1;
        const int ta = 8 * (16 * (w & 1) + (l >> 2)) + 4 * (w >> 1) + (l & 2);
        u32 oo = (u32)(ta + chb) * (DM * 2) + 2 * d0;
        const __amdgpu_buffer_rsrc_t ors = __builtin_amdgcn_make_buffer_rsrc(gout, 0, SEQ * DM * 2, 0x00020000);
        const LAS char* ym = sm + Y_OFF + chb * Y_CONV + 8 * (16 * w + (l >> 2)) + 2 * (l & 2);
        const LAS char* yc = sm + Y_OFF + chb * Y_CONV + 512 + ((w & 1) ? 128 * (w - 1) : 128 * (w + 1) - 1024) + 8 * (7 + (l < 12 ? (l & 12) : 0)) + 2 * (l & 2);
        const LAS char* xr = sm + XC_OFF + chb * XC_CONV + XC_PAD + 2 * (ta & ~7) + 2 * (6 - (ta & 7));
        float cmask = l < 12 ? 1.f : 0.f;
        u32 him = 0xffff0000u; asm volatile("" : "+s"(him));
        auto bfh = [&](u32 v) __attribute__((always_inline)) { return __uint_as_float(v & him); };
        u32 sel = chb ? 0x07060302u : 0x01000504u;
        const LAS char* gl = sm + TAB_OFF + 256 * w + 4 * l;
        asm volatile("" : "+v"(yb), "+v"(ym), "+v"(yc), "+v"(xr), "+v"(bb), "+v"(gl), "+v"(oo), "+v"(sel), "+v"(cmask), "+v"(tr0), "+v"(tr1), "+v"(zb) :: "memory");
        {
            f16v kd = {};
            typedef LAS s4v* trp;
            auto tr = [&](u32 a) __attribute__((always_inline)) { return __builtin_bit_cast(u2v, __builtin_amdgcn_ds_read_tr16_b64_v4i16((trp)(size_t)a)); };
            u4v fa[8], fb[8];
#pragma unroll
            for (int s = 0; s < 8 + 3; ++s) {
                if (s < 8) {
                    const u2v x0 = tr(tr0 + s * 1024), x1 = tr(tr1 + s * 1024), y0 = tr(tr0 + 8192 + s * 1024), y1 = tr(tr1 + 8192 + s * 1024);
                    fa[s] = u4v{x0[0], x0[1], x1[0], x1[1]}; fb[s] = u4v{y0[0], y0[1], y1[0], y1[1]};
                }
                __builtin_amdgcn_sched_barrier(0);
                if (s >= 3) kd = __builtin_amdgcn_mfma_f32_32x32x16_bf16(__builtin_bit_cast(bf8, fa[s - 3]), __builtin_bit_cast(bf8, fb[s - 3]), kd, 0, 0, 0);
                __builtin_amdgcn_sched_barrier(0);
            }
            volatile LAS u32* pairf = (volatile LAS u32*)(sm + FL_OFF + 16 + 4 * (w & 1));
            auto zw = [&](int off, const u4v& v) __attribute__((always_inline)) { *(LAS u4v*)(size_t)(zb + off) = v; };
            if (w >= 2) {
#pragma unroll
                for (int g = 0; g < 4; ++g) *(LAS f4v*)(tab + g * 1024 + l * 16) = f4v{kd[4 * g], kd[4 * g + 1], kd[4 * g + 2], kd[4 * g + 3]};
                asm volatile("" ::: "memory");
                if (l == 0) *pairf = 1u;
                asm volatile("s_waitcnt vmcnt(0)" ::: "memory");
                u4v c[4]; u32 pv[4];
#pragma unroll
                for (int pc = 0; pc < 4; ++pc) { c[pc] = *(const LAS u4v*)(size_t)(zb + 1024 * pc); pv[pc] = *(const LAS u32*)(size_t)(zb + 1024 * pc - 4); }
                if (l == 0) {
                    c[0][0] = (c[0][0] & 0xffff0000u) | (pk2(bf2f(c[0][0]) + bf2f(skip), 0.f) & 0xffffu);
                    *(LAS u32*)(size_t)zb = c[0][0];
                }
#pragma unroll
                for (int pc = 0; pc < 4; ++pc)
                    zw(Z_COPY + 1024 * pc, u4v{__builtin_amdgcn_alignbit(c[pc][0], pv[pc], 16), __builtin_amdgcn_alignbit(c[pc][1], c[pc][0], 16),
                                               __builtin_amdgcn_alignbit(c[pc][2], c[pc][1], 16), __builtin_amdgcn_alignbit(c[pc][3], c[pc][2], 16)});
            } else {
#pragma nounroll
                for (u32 spins = 0; spins < SPIN_CAP; ++spins) if (*pairf != 0u) break;
                asm volatile("" ::: "memory");
#pragma unroll
                for (int g = 0; g < 4; ++g) {
                    const f4v o = *(LAS f4v*)(tab + 32768 + g * 1024 + l * 16);
                    kd[4 * g] += o[0]; kd[4 * g + 1] += o[1]; kd[4 * g + 2] += o[2]; kd[4 * g + 3] += o[3];
                }
                if (l == 0) kd[0] += bf2f(skip);
                const float p7 = dppz<0x138>(kd[7]);
                zw(0, u4v{pk2(kd[0], kd[1]), pk2(kd[2], kd[3]), pk2(kd[4], kd[5]), pk2(kd[6], kd[7])});
                zw(Z_COPY, u4v{pk2(p7, kd[0]), pk2(kd[1], kd[2]), pk2(kd[3], kd[4]), pk2(kd[5], kd[6])});
                zw(1024, u4v{pk2(kd[8], kd[8]), pk2(kd[8], kd[9]), pk2(kd[9], kd[9]), pk2(kd[10], kd[10])});
                zw(Z_COPY + 1024, u4v{pk2(kd[8], kd[8]), pk2(kd[8], kd[8]), pk2(kd[9], kd[9]), pk2(kd[9], kd[10])});
#pragma unroll
                for (int a = 0; a < 2; ++a) {
                    const float lo = kd[11 + 2 * a], hi = kd[12 + 2 * a], tie = (l & 1) ? hi : lo;
                    const u32 ll = pk2(lo, lo), hh = pk2(hi, hi);
                    zw(2048 + 1024 * a, u4v{ll, ll, pk2(tie, hi), hh});
                    zw(Z_COPY + 2048 + 1024 * a, u4v{ll, ll, pk2(lo, tie), hh});
                }
            }
        }

        u4v A[32], Br[QRES + 4], Bs[8];
        f16v acc[2];
        u32 W0, W1, T0, T1, S0, S1;
        u32 uu[8], G2[8], yv[8][4], P[8]; float x[8][8], cf[8][6];
#define PIN(v) asm volatile("" : "+v"(v))
        auto ldA = [&](int i, int h) __attribute__((always_inline)) {
            const LAS u32* p = (const LAS u32*)(size_t)abase[i >> 3] + 32 * (i & 7) + 2 * h;
            A[i][2 * h] = p[0]; A[i][2 * h + 1] = p[1];
        };
        auto ldB = [&](int q) __attribute__((always_inline)) { return *(const LAS u4v*)(bb + 128 * q); };
        auto post = [&](int be, int n) __attribute__((always_inline)) {
            f16v& a = acc[be & 1];
            if (n < 12) { const int e = 8 - 4 * (n >> 2) + (n & 3); float t = a[e] + dppz<0x111>(a[e + 4]); PIN(t); a[e] = t; }
            else if (n == 12) { W0 = pk2(a[12], a[13]); PIN(W0); }
            else if (n == 13) { W1 = pk2(a[14], a[15]); PIN(W1); }
            else if (n == 14) { T0 = pk2(a[8], a[9]); PIN(T0); }
            else if (n == 15) { T1 = pk2(a[10], a[11]); PIN(T1); }
            else if (n == 16) { S0 = pk2(a[0], a[1]); PIN(S0); }
            else if (n == 17) { S1 = pk2(a[2], a[3]); PIN(S1); }
            else if (n == 18) *(LAS u2v*)(yb + 1024 * be) = u2v{S0, S1};
            else if (n == 19) { W0 = (u32)__builtin_amdgcn_update_dpp((int)W0, (int)T0, 0x104, 0xf, 0x4, false); PIN(W0); }
            else if (n == 20) { W1 = (u32)__builtin_amdgcn_update_dpp((int)W1, (int)T1, 0x104, 0xf, 0x4, false); PIN(W1); }
            else if (n == 21) { T0 = pk2(a[4], a[5]); PIN(T0); }
            else if (n == 22) { T1 = pk2(a[6], a[7]); PIN(T1); }
            else if (n == 23) { W0 = (u32)__builtin_amdgcn_update_dpp((int)W0, (int)T0, 0x108, 0xf, 0x2, false); PIN(W0); }
            else if (n == 24) { W1 = (u32)__builtin_amdgcn_update_dpp((int)W1, (int)T1, 0x108, 0xf, 0x2, false); PIN(W1); }
            else if (n == 25) *(LAS u2v*)(yb + 512 + 1024 * be) = u2v{W0, W1};
        };
        auto rd = [&](int be, int n) __attribute__((always_inline)) {
            if (n == 1) { yv[be][0] = *(const LAS u32*)(ym + Y_BR + 1024 * be); yv[be][2] = *(const LAS u32*)(ym + 1024 * be); }
            else if (n == 2) { yv[be][1] = *(const LAS u32*)(yc + Y_BR + 1024 * be); yv[be][3] = *(const LAS u32*)(yc + 1024 * be); }
            else if (n == 5) uu[be] = *(const volatile LAS u32*)(xr + 512 * be);
            else if (n == 6) G2[be] = *(const volatile LAS u32*)(gl + 1024 * be);
        };
        auto mix = [&](int be, int n) __attribute__((always_inline)) {
            float* c = cf[be]; float* y = x[be];
            if (n < 7) rd(be, n);
            else if (n == 9) { c[0] = bf2f(G2[be]); PIN(c[0]); }
            else if (n == 10) { c[1] = bfh(G2[be]); PIN(c[1]); }
            else if (n == 11) { c[4] = bfh(uu[be]); PIN(c[4]); }
            else if (n == 12) { c[2] = 1.f - c[0]; PIN(c[2]); }
            else if (n == 13) { c[3] = 1.f - c[1]; PIN(c[3]); }
            else if (n == 14) { c[5] = bf2f(uu[be]); PIN(c[5]); }
            else if (n == 15) { c[2] *= c[4]; PIN(c[2]); }
            else if (n == 16) { c[3] *= c[5]; PIN(c[3]); }
            else if (n < 25) { const int k = n - 17; if (be == 0 && (k == 2 || k == 6)) { if (!(w & 1)) yv[0][k >> 1] = 0u; } y[k] = (k & 1) ? bfh(yv[be][k >> 1]) : bf2f(yv[be][k >> 1]); PIN(y[k]); }
            else if (n < 29) { const int k = ((n - 25) & 1) + 4 * ((n - 25) >> 1); y[k] += cmask * y[k + 2]; PIN(y[k]); }
            else if (n < 31) { y[n - 29] *= c[n - 27]; PIN(y[n - 29]); }
            else if (n < 33) { y[n - 31] += c[n - 31] * y[n - 27]; if (n == 31) PIN(y[0]); }
            else if (n == 33) { P[be] = pk2(y[0], y[1]); PIN(P[be]); }
            else if (n == 34) { uu[be] = (u32)__builtin_amdgcn_update_dpp(0, (int)P[be], 0xB1, 0xf, 0xf, true); }
            else if (n == 35) { P[be] = __builtin_amdgcn_perm(P[be], uu[be], sel); PIN(P[be]); }
            else if (n == 36) __builtin_amdgcn_raw_buffer_store_b32(P[be], ors, oo, be * (256 * DM * 2), 0);
        };
        asm volatile("" ::: "memory");
#pragma unroll
        for (int i = 0; i < APRE; ++i) { ldA(i, 0); ldA(i, 1); }
#pragma unroll
        for (int i = 0; i < APRE; ++i) asm volatile("" : "+v"(A[i]));
        asm volatile("" ::: "memory");
        __builtin_amdgcn_s_barrier();
        asm volatile("" ::: "memory");
#pragma unroll
        for (int q = -3; q <= 2; ++q) Br[q + 3] = ldB(q);
        sfor<0, 8>([&](auto BE) __attribute__((always_inline)) {
            constexpr int be = BE.v;
            sfor<0, 4 * be + 4>([&](auto S) __attribute__((always_inline)) {
                constexpr int st = S.v;
                constexpr int q = (4 * be + 3 < APRE) ? st - 3 : (st < 4 * be ? st + 1 : st - 4 * be - 3);
                constexpr int i = 4 * be - q;
                const u4v bfr = (q > QRES) ? Bs[q & 7] : Br[q > QRES ? 0 : q + 3];
                if (st == 0) acc[be & 1] = __builtin_amdgcn_mfma_f32_32x32x16_bf16(__builtin_bit_cast(bf8, A[i]), __builtin_bit_cast(bf8, bfr), f16v{}, 0, 0, 0);
                else acc[be & 1] = __builtin_amdgcn_mfma_f32_32x32x16_bf16(__builtin_bit_cast(bf8, A[i]), __builtin_bit_cast(bf8, bfr), acc[be & 1], 0, 0, 0);
                __builtin_amdgcn_sched_barrier(0);
                constexpr int G = 2 * be * (be + 1) + st;
                sfor<0, SCH_W>([&](auto J) __attribute__((always_inline)) {
                    constexpr int e = SCH[G][J.v], kind = e >> 12, pa = (e >> 6) & 63, pb = e & 63;
                    if constexpr (kind == 1) ldA(pa, pb);
                    if constexpr (kind == 2) Br[pa] = ldB(pa - 3);
                    if constexpr (kind == 3) Bs[pa & 7] = ldB(pa);
                    if constexpr (kind == 4) post(pa, pb);
                    if constexpr (kind == 5) mix(pa, pb);
                    if constexpr (kind == 6) {
                        asm volatile("s_waitcnt lgkmcnt(%0)\n\ts_barrier" :: "i"(pb) : "memory");
                        __builtin_amdgcn_sched_barrier(0);
                    }
                    if constexpr (kind == 0 && be > 0 && st < 2) __builtin_amdgcn_s_setprio(0);
                });
                __builtin_amdgcn_sched_barrier(0);
            });
        });
        {
            constexpr int ord[26] = {0, 1, 2, 3, 12, 13, 4, 5, 6, 7, 14, 15, 8, 9, 10, 11, 19, 20, 21, 22, 16, 17, 23, 24, 18, 25};
            sfor<0, 26>([&](auto J) __attribute__((always_inline)) { post(7, ord[J.v]); });
        }
        asm volatile("s_waitcnt lgkmcnt(0)" ::: "memory");
        __builtin_amdgcn_s_barrier();
        asm volatile("" ::: "memory");
        sfor<1, 3>([&](auto J) __attribute__((always_inline)) { mix(7, J.v); });
        sfor<17, 37>([&](auto J) __attribute__((always_inline)) { mix(7, J.v); });
        if (blockIdx.x == 0 && tid == 0) __hip_atomic_store((gu64*)ws, tag, RLX_AGENT);
    }

}

extern "C" void kernel_launch(void* const* d_in, const int* in_sizes, int n_in,
                              void* d_out, int out_size, void* d_ws, size_t ws_size,
                              hipStream_t stream) {
    fused_k<<<256, 512, 0, stream>>>((const u16*)d_in[0], (const u16*)d_in[1], (const u16*)d_in[2], (const u16*)d_in[3], (const u16*)d_in[4],
             (const u16*)d_in[5], (const u16*)d_in[6], (const u16*)d_in[8], (const u16*)d_in[9], (const u16*)d_in[10],
             (const u16*)d_in[11], (const u16*)d_in[12], (const u16*)d_in[13], (u16*)d_out, (char*)d_ws);
}
```
